```python
import torch

def _softmax(x, axis=-1):
    return torch.softmax(x, dim=axis)
def _gelu(x):
    return torch.nn.functional.gelu(x, approximate="tanh")
def _silu(x):
    return torch.nn.functional.silu(x)
def _sigmoid(x):
    return torch.sigmoid(x)
def _layernorm(x, axis=-1, eps=1e-5):
    m = x.mean(dim=axis, keepdim=True)
    v = x.var(dim=axis, keepdim=True, unbiased=False)
    return (x - m) / torch.sqrt(v + eps)
def _scatter_add(tgt, idx, src, axis=0):
    return tgt.clone().index_add_(axis, idx, src)
def _segment_sum(src, seg, n):
    out = torch.zeros((n,) + src.shape[1:], dtype=src.dtype)
    return out.index_add_(0, seg, src)
def _pad(x, pw):
    flat = tuple(v for pair in reversed(pw) for v in pair)
    return torch.nn.functional.pad(x, flat)

kernel_name = 'sequential_preLN__sliding_block_cached__dense_activation_gelu__decode'

def setup_inputs():

    _w = (lambda a: (lambda t: t.to(torch.bfloat16) if t.is_floating_point() else t)(torch.as_tensor(a)))
    def _A_setup():
        B, Sq, D = 1, 1, 512
        Hq, Hkv, Sk, Dh = 32, 32, 16384, 128
        assert Hq % Hkv == 0, (Hq, Hkv)
        return {
            "x": _w((lambda *s: torch.randn(*s, dtype=torch.bfloat16) * 0.1)(B, Sq, D)),
            "k_cache": _w((lambda *s: torch.randn(*s, dtype=torch.bfloat16) * 1.6)(B, Hkv, Sk, Dh)),
            "v_cache": _w((lambda *s: torch.randn(*s, dtype=torch.bfloat16) * 1.6)(B, Hkv, Sk, Dh)),
            "wq": _w((lambda *s: torch.randn(*s, dtype=torch.bfloat16) * 1.6)(D, Hq * Dh) * (D ** -0.5)),
            "wo": _w((lambda *s: torch.randn(*s, dtype=torch.bfloat16) * 1.6)(Hq * Dh, D) * ((Hq * Dh) ** -0.5)),
        }
    _A_in = _A_setup()

    def _B_setup():
        B, S, D, F = 1, 1, 512, 1376
        return {
            "x": _w((lambda *s: torch.randn(*s, dtype=torch.bfloat16) * 0.1)(B, S, D)),
            "w1": _w((lambda *s: torch.randn(*s, dtype=torch.bfloat16) * 3.2)(D, F) * (D ** -0.5)),
            "b1": _w((lambda *s: torch.randn(*s, dtype=torch.bfloat16) * 3.2)(F) * 0.01),
            "w2": _w((lambda *s: torch.randn(*s, dtype=torch.bfloat16) * 3.2)(F, D) * (F ** -0.5)),
            "b2": _w((lambda *s: torch.randn(*s, dtype=torch.bfloat16) * 3.2)(D) * 0.01),
        }
    _B_in = _B_setup()

    return {
        "A_x": _A_in["x"],
        "A_k_cache": _A_in["k_cache"],
        "A_v_cache": _A_in["v_cache"],
        "A_wq": _A_in["wq"],
        "A_wo": _A_in["wo"],
        "B_x": _B_in["x"],
        "B_w1": _B_in["w1"],
        "B_b1": _B_in["b1"],
        "B_w2": _B_in["w2"],
        "B_b2": _B_in["b2"],
    }

def reference(A_x, A_k_cache, A_v_cache, A_wq, A_wo, B_x, B_w1, B_b1, B_w2, B_b2):
    def _A_ref(x, k_cache, v_cache, wq, wo):
        B, Sq, D = x.shape
        Hq, Dh = 32, 128
        Hkv, Sk = k_cache.shape[1], k_cache.shape[2]
        W = min(4096, max(256, Sk // 4))
        rep = Hq // Hkv
        q = (lambda x, *d: x.permute(*d))((x @ wq).reshape(B, Sq, Hq, Dh), 0, 2, 1, 3)
        k = (lambda x, n, axis: torch.repeat_interleave(x, n, dim=axis))(k_cache[:, :, Sk - W:, :], rep, axis=1)
        v = (lambda x, n, axis: torch.repeat_interleave(x, n, dim=axis))(v_cache[:, :, Sk - W:, :], rep, axis=1)
        scores = torch.einsum("bhqd,bhkd->bhqk", q, k) * (Dh ** -0.5)
        p = _softmax(scores, axis=-1)
        o = torch.einsum("bhqk,bhkd->bhqd", p, v)
        return (lambda x, *d: x.permute(*d))(o, 0, 2, 1, 3).reshape(B, Sq, Hq * Dh) @ wo

    def _B_ref(x, w1, b1, w2, b2):
        h = torch.einsum("bsd,df->bsf", x, w1) + b1
        h = _gelu(h)
        return torch.einsum("bsf,fd->bsd", h, w2) + b2

    x = A_x
    h = x + _A_ref(_layernorm(x, axis=-1), A_k_cache, A_v_cache, A_wq, A_wo)
    h2 = h + _B_ref(_layernorm(h, axis=-1), B_w1, B_b1, B_w2, B_b2)
    return h2
```

```cpp
#include <hip/hip_runtime.h>
#include <stdint.h>

#define GAS __attribute__((address_space(1)))
#define LAS __attribute__((address_space(3)))
typedef unsigned v4u __attribute__((ext_vector_type(4)));
typedef unsigned v2u __attribute__((ext_vector_type(2)));
typedef float f32x4 __attribute__((ext_vector_type(4)));
typedef float f32x2 __attribute__((ext_vector_type(2)));
typedef __bf16 bf16x2 __attribute__((ext_vector_type(2)));

constexpr int D = 512, H = 32, DH = 128, SK = 16384, WIN = 4096, FF = 1376;
constexpr int NSPLIT = 8, KEYS = WIN / NSPLIT;
constexpr int NW1 = FF / 32;
constexpr float EPS = 1e-5f;
constexpr float C2 = 0.088388347648318440f * 1.4426950408889634f;

constexpr size_t WS_PART = 8192;
constexpr size_t WS_ML = WS_PART + 256 * 512 * 4;
constexpr size_t WS_H = WS_ML + 256 * 2 * 4;
constexpr size_t WS_OP = WS_H + 512 * 4;

__device__ __forceinline__ float bflo(unsigned u) { return __builtin_bit_cast(float, u << 16); }
__device__ __forceinline__ float bfhi(unsigned u) { return __builtin_bit_cast(float, u & 0xffff0000u); }
__device__ __forceinline__ float bf2f(unsigned short b) { return __builtin_bit_cast(float, (unsigned)b << 16); }
__device__ __forceinline__ unsigned cvtpk(float lo, float hi) {
  bf16x2 r = {(__bf16)lo, (__bf16)hi};
  return __builtin_bit_cast(unsigned, r);
}
__device__ __forceinline__ float rbf(float x) { return bflo(cvtpk(x, x)); }
__device__ __forceinline__ float dot2(unsigned k, unsigned q, float acc) {
  return __builtin_amdgcn_fdot2_f32_bf16(__builtin_bit_cast(bf16x2, k), __builtin_bit_cast(bf16x2, q), acc, false);
}
template <int CTRL> __device__ __forceinline__ float dpp(float x) {
  return __builtin_bit_cast(float, __builtin_amdgcn_mov_dpp(__builtin_bit_cast(int, x), CTRL, 0xf, 0xf, true));
}
constexpr int XOR1 = 0xB1, XOR2 = 0x4E, XOR7 = 0x141, ROR8 = 0x128, ROR4 = 0x124, BCAST = 0x150;
__device__ __forceinline__ float swap16_add(float a, float b) {
  auto s = __builtin_amdgcn_permlane16_swap(__float_as_uint(a), __float_as_uint(b), false, false);
  return __uint_as_float(s[0]) + __uint_as_float(s[1]);
}
__device__ __forceinline__ float swap32_add(float a, float b) {
  auto s = __builtin_amdgcn_permlane32_swap(__float_as_uint(a), __float_as_uint(b), false, false);
  return __uint_as_float(s[0]) + __uint_as_float(s[1]);
}
__device__ __forceinline__ float swap16_max(float a) {
  auto s = __builtin_amdgcn_permlane16_swap(__float_as_uint(a), __float_as_uint(a), false, false);
  return fmaxf(__uint_as_float(s[0]), __uint_as_float(s[1]));
}
__device__ __forceinline__ float swap32_max(float a) {
  auto s = __builtin_amdgcn_permlane32_swap(__float_as_uint(a), __float_as_uint(a), false, false);
  return fmaxf(__uint_as_float(s[0]), __uint_as_float(s[1]));
}
__device__ __forceinline__ float row_sum(float x) {
  x += dpp<XOR1>(x); x += dpp<XOR2>(x); x += dpp<XOR7>(x); x += dpp<ROR8>(x); return x;
}
__device__ __forceinline__ float wave_sum(float x) { x = row_sum(x); x = swap16_add(x, x); return swap32_add(x, x); }
__device__ __forceinline__ float wave_max(float x) {
  x = fmaxf(x, dpp<XOR1>(x)); x = fmaxf(x, dpp<XOR2>(x)); x = fmaxf(x, dpp<XOR7>(x)); x = fmaxf(x, dpp<ROR8>(x));
  x = swap16_max(x); return swap32_max(x);
}
__device__ __forceinline__ void fma8(float (&a)[8], v4u w, float p) {
#pragma unroll
  for (int j = 0; j < 4; ++j) { a[2 * j] = fmaf(p, bflo(w[j]), a[2 * j]); a[2 * j + 1] = fmaf(p, bfhi(w[j]), a[2 * j + 1]); }
}
template <int I> struct PV {
  static __device__ __forceinline__ void run(float (&a)[8], const v4u (&w)[16], float p) {
    fma8(a, w[I], dpp<BCAST + I>(p));
    PV<I + 1>::run(a, w, p);
  }
};
template <> struct PV<16> { static __device__ __forceinline__ void run(float (&)[8], const v4u (&)[16], float) {} };
__device__ __forceinline__ f32x2 rows_reduce8(const float (&a)[8]) {
  float u[4];
#pragma unroll
  for (int j = 0; j < 4; ++j) u[j] = swap16_add(a[j], a[j + 4]);
  f32x2 t;
  t.x = swap32_add(u[0], u[2]);
  t.y = swap32_add(u[1], u[3]);
  return t;
}
__device__ __forceinline__ v4u ldnt(const void* p) { return __builtin_nontemporal_load((const GAS v4u*)p); }
__device__ __forceinline__ v4u ld(const void* p) { return *(const GAS v4u*)p; }
#define SB() __builtin_amdgcn_sched_barrier(0)
__device__ __forceinline__ void bar() {
  asm volatile("s_waitcnt lgkmcnt(0)" ::: "memory");
  __builtin_amdgcn_s_barrier();
  asm volatile("" ::: "memory");
}

struct Args {
  const unsigned short *x, *kc, *vc, *wq, *wo, *w1, *b1, *w2, *b2;
  unsigned short* out;
  unsigned char* ws;
};

__device__ __forceinline__ void ln_stats(const float (&v)[8], float& mean, float& rstd) {
  float s = ((v[0] + v[1]) + (v[2] + v[3])) + ((v[4] + v[5]) + (v[6] + v[7]));
  mean = wave_sum(s) * (1.0f / D);
  float q = 0.f;
#pragma unroll
  for (int j = 0; j < 8; ++j) { const float d = v[j] - mean; q = fmaf(d, d, q); }
  const float var = wave_sum(q) * (1.0f / D);
  rstd = 1.0f / sqrtf(var + EPS);
}

__global__ void __launch_bounds__(512, 2) k_attn(Args a) {
  __shared__ __attribute__((aligned(16))) float lds[8 * 512 + 64];
  const int b = blockIdx.x, tid = threadIdx.x, lane = tid & 63, w = __builtin_amdgcn_readfirstlane(tid >> 6);
  const int r = lane >> 4, c = lane & 15;
  const int head = (b & 7) * 4 + (b >> 6), split = (b >> 3) & 7, P = head * 8 + split;
  const v4u xa = ld(a.x + 8 * lane);
  const unsigned short xs = a.x[64 * w + 4 * c + r];
  SB();
  v4u wq[16], kr[16], wo[16], vr[16];
  {
    const unsigned short* p = a.wq + (size_t)(64 * w + r) * (H * DH) + head * DH + 8 * c;
#pragma unroll
    for (int i = 0; i < 16; ++i) wq[i] = ld(p + (size_t)i * 4 * (H * DH));
  }
  SB();
  const size_t kvoff = ((size_t)head * SK + (SK - WIN) + split * KEYS + 64 * w) * DH + 8 * lane;
#pragma unroll
  for (int i = 0; i < 16; ++i) kr[i] = ldnt(a.kc + kvoff + i * 512);
  SB();
  {
    const unsigned short* p = a.wo + (size_t)(head * DH + 16 * w) * D + 8 * lane;
#pragma unroll
    for (int i = 0; i < 16; ++i) wo[i] = ld(p + i * D);
  }
  SB();
  float xv[8];
#pragma unroll
  for (int j = 0; j < 4; ++j) { xv[2 * j] = bflo(xa[j]); xv[2 * j + 1] = bfhi(xa[j]); }
  float mean, rstd;
  ln_stats(xv, mean, rstd);
  const float xn = rbf((bf2f(xs) - mean) * rstd);
  float acc[8];
#pragma unroll
  for (int j = 0; j < 8; ++j) acc[j] = 0.f;
  PV<0>::run(acc, wq, xn);
  SB();
#pragma unroll
  for (int i = 0; i < 16; ++i) vr[i] = ldnt(a.vc + kvoff + i * 512);
  SB();
  const int doff = 8 * c + ((r & 1) << 2) + (r & 2);
  {
    const f32x2 t = rows_reduce8(acc);
    *(f32x2*)&lds[w * 128 + doff] = t;
  }
  bar();
  unsigned qp[4];
  {
    f32x4 s0 = {0.f, 0.f, 0.f, 0.f}, s1 = {0.f, 0.f, 0.f, 0.f};
#pragma unroll
    for (int ww = 0; ww < 8; ++ww) { s0 += *(const f32x4*)&lds[ww * 128 + 8 * c]; s1 += *(const f32x4*)&lds[ww * 128 + 8 * c + 4]; }
    qp[0] = cvtpk(s0.x, s0.y); qp[1] = cvtpk(s0.z, s0.w); qp[2] = cvtpk(s1.x, s1.y); qp[3] = cvtpk(s1.z, s1.w);
  }
  float x[16];
#pragma unroll
  for (int i = 0; i < 16; ++i) {
    float d = 0.f;
#pragma unroll
    for (int j = 0; j < 4; ++j) d = dot2(kr[i][j], qp[j], d);
    x[i] = d;
  }
#define TR_STEP(HALF, CTRL, BIT) _Pragma("unroll") for (int h = 0; h < (HALF); ++h) { \
    const float lo_ = x[h], hi_ = x[h + (HALF)]; const float t1_ = lo_ + dpp<CTRL>(lo_), t2_ = hi_ + dpp<CTRL>(hi_); \
    x[h] = (c & (BIT)) ? t2_ : t1_; }
  TR_STEP(8, ROR8, 8) TR_STEP(4, XOR7, 4) TR_STEP(2, XOR2, 2) TR_STEP(1, XOR1, 1)
#undef TR_STEP
  const float sc = x[0] * C2;
  const float mw = wave_max(sc);
  float* lm = lds + 1024;
  if (lane == 0) lm[w] = mw;
  bar();
  float M = lm[0];
#pragma unroll
  for (int ww = 1; ww < 8; ++ww) M = fmaxf(M, lm[ww]);
  const float p = __builtin_amdgcn_exp2f(sc - M);
  const float lw = wave_sum(p);
  if (lane == 0) lm[8 + w] = lw;
#pragma unroll
  for (int j = 0; j < 8; ++j) acc[j] = 0.f;
  PV<0>::run(acc, vr, p);
  {
    const f32x2 t = rows_reduce8(acc);
    *(f32x2*)&lds[2048 + w * 128 + doff] = t;
  }
  bar();
  float o = lds[2048 + r * 128 + 16 * w + c] + lds[2048 + (r + 4) * 128 + 16 * w + c];
  o = swap16_add(o, o);
  o = swap32_add(o, o);
  float L = 0.f;
  if (tid == 0) {
#pragma unroll
    for (int ww = 0; ww < 8; ++ww) L += lm[8 + ww];
  }
#pragma unroll
  for (int j = 0; j < 8; ++j) acc[j] = 0.f;
  PV<0>::run(acc, wo, o);
  bar();
  *(f32x4*)&lds[w * 512 + 8 * lane] = f32x4{acc[0], acc[1], acc[2], acc[3]};
  *(f32x4*)&lds[w * 512 + 8 * lane + 4] = f32x4{acc[4], acc[5], acc[6], acc[7]};
  bar();
  float s = 0.f;
#pragma unroll
  for (int ww = 0; ww < 8; ++ww) s += lds[ww * 512 + tid];
  ((float*)(a.ws + WS_PART))[P * 512 + tid] = s;
  if (tid == 0) *(f32x2*)(a.ws + WS_ML + P * 8) = f32x2{M, L};
}

__global__ void __launch_bounds__(64) k_h(Args a) {
  const int j = blockIdx.x, lane = threadIdx.x;
  const float* part = (const float*)(a.ws + WS_PART);
  f32x4 v[4]; f32x2 ml[4];
#pragma unroll
  for (int i = 0; i < 4; ++i) { v[i] = *(const f32x4*)(part + (64 * i + lane) * 512 + 4 * j); ml[i] = *(const f32x2*)(a.ws + WS_ML + (64 * i + lane) * 8); }
  f32x4 s = {0.f, 0.f, 0.f, 0.f};
#pragma unroll
  for (int i = 0; i < 4; ++i) {
    float M = ml[i].x;
    M = fmaxf(M, dpp<XOR1>(M)); M = fmaxf(M, dpp<XOR2>(M)); M = fmaxf(M, dpp<XOR7>(M));
    const float e = __builtin_amdgcn_exp2f(ml[i].x - M);
    float L = e * ml[i].y;
    L += dpp<XOR1>(L); L += dpp<XOR2>(L); L += dpp<XOR7>(L);
    const float wgt = e / L;
    s += wgt * v[i];
  }
  float u0 = swap32_add(s.x, s.z), u1 = swap32_add(s.y, s.w);
  float t = swap16_add(u0, u1);
  t = row_sum(t);
  const int r = lane >> 4;
  if ((lane & 15) == 0) {
    const int col = 4 * j + r;
    const float h = rbf(bf2f(a.x[col]) + rbf(t));
    ((float*)(a.ws + WS_H))[col] = h;
  }
}

__device__ __forceinline__ float gelu_tanh(float x) {
  const float u = 0.7978845608028654f * (x + 0.044715f * x * x * x);
  const float e = __builtin_amdgcn_exp2f(u * 2.8853900817779268f);
  const float t = 1.0f - 2.0f / (e + 1.0f);
  return 0.5f * x * (1.0f + t);
}

__global__ void __launch_bounds__(512, 2) k_mlp(Args a) {
  __shared__ __attribute__((aligned(16))) float lds[8 * 512 + 256];
  const int g = blockIdx.x, tid = threadIdx.x, lane = tid & 63, w = __builtin_amdgcn_readfirstlane(tid >> 6);
  const int rg = lane >> 2, q = lane & 3;
  v4u w1[4], w2[4];
#pragma unroll
  for (int i = 0; i < 4; ++i) w1[i] = ld(a.w1 + (size_t)(64 * w + 16 * i + rg) * FF + 32 * g + 8 * q);
#pragma unroll
  for (int i = 0; i < 4; ++i) w2[i] = ld(a.w2 + (size_t)(32 * g + 4 * w + i) * D + 8 * lane);
  const float b1 = bf2f(a.b1[32 * g + 4 * w + (lane & 3)]);
  const float* hb = (const float*)(a.ws + WS_H);
  const f32x4 h0 = *(const f32x4*)(hb + 8 * lane), h1 = *(const f32x4*)(hb + 8 * lane + 4);
  float hr[4];
#pragma unroll
  for (int i = 0; i < 4; ++i) hr[i] = hb[64 * w + 16 * i + rg];
  float hv[8] = {h0.x, h0.y, h0.z, h0.w, h1.x, h1.y, h1.z, h1.w};
  float mean, rstd;
  ln_stats(hv, mean, rstd);
  float acc[8];
#pragma unroll
  for (int j = 0; j < 8; ++j) acc[j] = 0.f;
#pragma unroll
  for (int i = 0; i < 4; ++i) fma8(acc, w1[i], rbf((hr[i] - mean) * rstd));
  float u[4];
#pragma unroll
  for (int j = 0; j < 4; ++j) u[j] = swap32_add(acc[j], acc[j + 4]);
  float t0 = swap16_add(u[0], u[2]), t1 = swap16_add(u[1], u[3]);
  t0 += dpp<ROR8>(t0); t0 += dpp<ROR4>(t0);
  t1 += dpp<ROR8>(t1); t1 += dpp<ROR4>(t1);
  float* up = lds + 4096;
  if ((lane & 12) == 0) *(f32x2*)&up[w * 32 + 8 * q + 2 * (lane >> 4)] = f32x2{t0, t1};
  bar();
  float uu = up[((lane >> 2) & 7) * 32 + 4 * w + (lane & 3)];
  uu += dpp<ROR8>(uu); uu += dpp<ROR4>(uu);
  uu = swap16_add(uu, uu);
  const float gv = rbf(gelu_tanh(rbf(rbf(uu) + b1)));
#pragma unroll
  for (int j = 0; j < 8; ++j) acc[j] = 0.f;
  fma8(acc, w2[0], dpp<0x00>(gv));
  fma8(acc, w2[1], dpp<0x55>(gv));
  fma8(acc, w2[2], dpp<0xAA>(gv));
  fma8(acc, w2[3], dpp<0xFF>(gv));
  *(f32x4*)&lds[w * 512 + 8 * lane] = f32x4{acc[0], acc[1], acc[2], acc[3]};
  *(f32x4*)&lds[w * 512 + 8 * lane + 4] = f32x4{acc[4], acc[5], acc[6], acc[7]};
  bar();
  float s = 0.f;
#pragma unroll
  for (int ww = 0; ww < 8; ++ww) s += lds[ww * 512 + tid];
  ((float*)(a.ws + WS_OP))[g * 512 + tid] = s;
}

__global__ void __launch_bounds__(64) k_out(Args a) {
  const int j = blockIdx.x, lane = threadIdx.x, rg = lane >> 3, q = lane & 7;
  const float* op = (const float*)(a.ws + WS_OP);
  f32x4 s = {0.f, 0.f, 0.f, 0.f};
#pragma unroll
  for (int i = 0; i < 6; ++i) {
    const int row = 8 * i + rg;
    if (row < NW1) s += *(const f32x4*)(op + row * 512 + 32 * j + 4 * q);
  }
  float u0 = swap32_add(s.x, s.z), u1 = swap32_add(s.y, s.w);
  float t = swap16_add(u0, u1);
  t += dpp<ROR8>(t);
  const int r = lane >> 4;
  if ((lane & 8) == 0) {
    const int col = 32 * j + 4 * q + r;
    const float h = ((const float*)(a.ws + WS_H))[col];
    const float y = rbf(rbf(t) + bf2f(a.b2[col]));
    a.out[col] = (unsigned short)(cvtpk(h + y, 0.f) & 0xffffu);
  }
}

extern "C" void kernel_launch(void* const* d_in, const int* in_sizes, int n_in, void* d_out, int out_size, void* d_ws,
                              size_t ws_size, hipStream_t stream) {
  Args a;
  a.x = (const unsigned short*)d_in[0];
  a.kc = (const unsigned short*)d_in[1];
  a.vc = (const unsigned short*)d_in[2];
  a.wq = (const unsigned short*)d_in[3];
  a.wo = (const unsigned short*)d_in[4];
  a.w1 = (const unsigned short*)d_in[6];
  a.b1 = (const unsigned short*)d_in[7];
  a.w2 = (const unsigned short*)d_in[8];
  a.b2 = (const unsigned short*)d_in[9];
  a.out = (unsigned short*)d_out;
  a.ws = (unsigned char*)d_ws;
  k_attn<<<256, 512, 0, stream>>>(a);
  k_h<<<128, 64, 0, stream>>>(a);
  k_mlp<<<NW1, 512, 0, stream>>>(a);
  k_out<<<16, 64, 0, stream>>>(a);
}
```

```cpp
#include <hip/hip_runtime.h>
#include <stdint.h>

#define GAS __attribute__((address_space(1)))
#define LAS __attribute__((address_space(3)))
typedef unsigned v4u __attribute__((ext_vector_type(4)));
typedef unsigned v2u __attribute__((ext_vector_type(2)));
typedef float f32x4 __attribute__((ext_vector_type(4)));
typedef float f32x2 __attribute__((ext_vector_type(2)));
typedef __bf16 bf16x2 __attribute__((ext_vector_type(2)));
typedef __amdgpu_buffer_rsrc_t rsrc_t;

constexpr int D = 512, H = 32, DH = 128, SK = 16384, WIN = 4096, FF = 1376;
constexpr int NL = 17;
constexpr int NW1 = FF / 32;
constexpr float EPS = 1e-5f;
constexpr float C2 = 0.088388347648318440f * 1.4426950408889634f;
constexpr unsigned SENT = 0xFFFFFFFFu;
constexpr unsigned SPIN_CAP = 1u << 18;
constexpr int AUX_NT = 2, AUX_SC1 = 16;

constexpr int WS_EPOCH = 0;
constexpr int WS_READY = 1024;
constexpr int WS_Q = 4096;
constexpr int WS_ACC = WS_Q + 4096 * 8;
constexpr int WS_ML = WS_ACC + 256 * 512;
constexpr int WS_PART = WS_ML + 256 * 16;
constexpr int WS_H = WS_PART + 32 * 2048;
constexpr int WS_OP = WS_H + 2048;
constexpr int WS_END = WS_OP + NW1 * 2048;

constexpr int L_XS = 0, L_QS = 512, L_FLAG = 576, L_LM = 592, L_REC = 640, L_OS = 1664, L_RED = 1792, L_UP = 2304, L_RED2 = 2560,
              L_END = L_RED2 + 4096;

__device__ __forceinline__ float bflo(unsigned u) { return __builtin_bit_cast(float, u << 16); }
__device__ __forceinline__ float bfhi(unsigned u) { return __builtin_bit_cast(float, u & 0xffff0000u); }
__device__ __forceinline__ float bf2f(unsigned short b) { return __builtin_bit_cast(float, (unsigned)b << 16); }
__device__ __forceinline__ unsigned cvtpk(float lo, float hi) {
  bf16x2 r = {(__bf16)lo, (__bf16)hi};
  return __builtin_bit_cast(unsigned, r);
}
__device__ __forceinline__ float rbf(float x) { return bflo(cvtpk(x, x)); }
__device__ __forceinline__ float dot2(unsigned k, unsigned q, float acc) {
  return __builtin_amdgcn_fdot2_f32_bf16(__builtin_bit_cast(bf16x2, k), __builtin_bit_cast(bf16x2, q), acc, false);
}
template <int CTRL> __device__ __forceinline__ float dpp(float x) {
  return __builtin_bit_cast(float, __builtin_amdgcn_mov_dpp(__builtin_bit_cast(int, x), CTRL, 0xf, 0xf, true));
}
constexpr int XOR1 = 0xB1, XOR2 = 0x4E, XOR7 = 0x141, ROR8 = 0x128, ROR4 = 0x124, BCAST = 0x150;
__device__ __forceinline__ float swap16_add(float a, float b) {
  auto s = __builtin_amdgcn_permlane16_swap(__float_as_uint(a), __float_as_uint(b), false, false);
  return __uint_as_float(s[0]) + __uint_as_float(s[1]);
}
__device__ __forceinline__ float swap32_add(float a, float b) {
  auto s = __builtin_amdgcn_permlane32_swap(__float_as_uint(a), __float_as_uint(b), false, false);
  return __uint_as_float(s[0]) + __uint_as_float(s[1]);
}
__device__ __forceinline__ float swap16_max(float a) {
  auto s = __builtin_amdgcn_permlane16_swap(__float_as_uint(a), __float_as_uint(a), false, false);
  return fmaxf(__uint_as_float(s[0]), __uint_as_float(s[1]));
}
__device__ __forceinline__ float swap32_max(float a) {
  auto s = __builtin_amdgcn_permlane32_swap(__float_as_uint(a), __float_as_uint(a), false, false);
  return fmaxf(__uint_as_float(s[0]), __uint_as_float(s[1]));
}
__device__ __forceinline__ float row_sum(float x) {
  x += dpp<XOR1>(x); x += dpp<XOR2>(x); x += dpp<XOR7>(x); x += dpp<ROR8>(x); return x;
}
__device__ __forceinline__ float wave_sum(float x) { x = row_sum(x); x = swap16_add(x, x); return swap32_add(x, x); }
__device__ __forceinline__ float wave_max(float x) {
  x = fmaxf(x, dpp<XOR1>(x)); x = fmaxf(x, dpp<XOR2>(x)); x = fmaxf(x, dpp<XOR7>(x)); x = fmaxf(x, dpp<ROR8>(x));
  x = swap16_max(x); return swap32_max(x);
}
__device__ __forceinline__ void fma8(float (&a)[8], v4u w, float p) {
#pragma unroll
  for (int j = 0; j < 4; ++j) { a[2 * j] = fmaf(p, bflo(w[j]), a[2 * j]); a[2 * j + 1] = fmaf(p, bfhi(w[j]), a[2 * j + 1]); }
}
template <int I> struct PV {
  static __device__ __forceinline__ void run(float (&a)[8], const v4u (&w)[NL], float p) {
    fma8(a, w[I], dpp<BCAST + I>(p));
    PV<I + 1>::run(a, w, p);
  }
};
template <> struct PV<16> { static __device__ __forceinline__ void run(float (&)[8], const v4u (&)[NL], float) {} };
__device__ __forceinline__ f32x2 rows_reduce8(const float (&a)[8]) {
  float u[4];
#pragma unroll
  for (int j = 0; j < 4; ++j) u[j] = swap16_add(a[j], a[j + 4]);
  f32x2 t;
  t.x = swap32_add(u[0], u[2]);
  t.y = swap32_add(u[1], u[3]);
  return t;
}
#define SB() __builtin_amdgcn_sched_barrier(0)
__device__ __forceinline__ void bar() {
  asm volatile("s_waitcnt lgkmcnt(0)" ::: "memory");
  __builtin_amdgcn_s_barrier();
  asm volatile("" ::: "memory");
}
__device__ __forceinline__ rsrc_t mkrs(const void* p, int bytes) { return __builtin_amdgcn_make_buffer_rsrc((void*)p, 0, bytes, 0x00020000); }
__device__ __forceinline__ v4u ld128(rsrc_t rs, int voff, int soff, int aux) {
  return aux == AUX_NT ? __builtin_amdgcn_raw_buffer_load_b128(rs, voff, soff, AUX_NT) : __builtin_amdgcn_raw_buffer_load_b128(rs, voff, soff, AUX_SC1);
}
__device__ __forceinline__ v4u ldx128(rsrc_t rs, int off) { return __builtin_amdgcn_raw_buffer_load_b128(rs, off, 0, AUX_SC1); }
__device__ __forceinline__ v2u ldx64(rsrc_t rs, int off) { return __builtin_amdgcn_raw_buffer_load_b64(rs, off, 0, AUX_SC1); }
__device__ __forceinline__ unsigned ldx32(rsrc_t rs, int off) { return __builtin_amdgcn_raw_buffer_load_b32(rs, off, 0, AUX_SC1); }
__device__ __forceinline__ void stx128(rsrc_t rs, int off, v4u v) { __builtin_amdgcn_raw_buffer_store_b128(v, rs, off, 0, AUX_SC1); }
__device__ __forceinline__ void stx64(rsrc_t rs, int off, v2u v) { __builtin_amdgcn_raw_buffer_store_b64(v, rs, off, 0, AUX_SC1); }
__device__ __forceinline__ void stx32(rsrc_t rs, int off, unsigned v) { __builtin_amdgcn_raw_buffer_store_b32(v, rs, off, 0, AUX_SC1); }
__device__ __forceinline__ unsigned pub(float x) { const unsigned u = __float_as_uint(x); return u < 0xFFFFFFFEu ? u : 0xFFFFFFFEu; }
__device__ __forceinline__ float asf(unsigned u) { return __uint_as_float(u); }
#define MEMFENCE() asm volatile("" ::: "memory")
__device__ __forceinline__ bool spin(unsigned& n) {
  __builtin_amdgcn_s_sleep(1);
  return ++n > SPIN_CAP;
}
__device__ __forceinline__ void lds_wait(volatile LAS unsigned* f) {
  _Pragma("unroll 1") for (unsigned n = 0; *f == 0u;) if (spin(n)) break;
  MEMFENCE();
}

#define STAMP(i)
#define STAMP_DECL
#define STAMP_OUT(idx)

struct Args {
  const unsigned short *x, *kc, *vc, *wq, *wo, *w1, *b1, *w2, *b2;
  unsigned short* out;
  unsigned char* ws;
};

__device__ __forceinline__ void ln_stats(const float (&v)[8], float& mean, float& rstd) {
  float s = ((v[0] + v[1]) + (v[2] + v[3])) + ((v[4] + v[5]) + (v[6] + v[7]));
  mean = wave_sum(s) * (1.0f / D);
  float q = 0.f;
#pragma unroll
  for (int j = 0; j < 8; ++j) { const float d = v[j] - mean; q = fmaf(d, d, q); }
  const float var = wave_sum(q) * (1.0f / D);
  rstd = 1.0f / sqrtf(var + EPS);
}
__device__ __forceinline__ float gelu_tanh(float x) {
  const float u = 0.7978845608028654f * (x + 0.044715f * x * x * x);
  const float e = __builtin_amdgcn_exp2f(u * 2.8853900817779268f);
  const float t = 1.0f - 2.0f / (e + 1.0f);
  return 0.5f * x * (1.0f + t);
}

__global__ void __launch_bounds__(512, 2) fused_block(Args a) {
  __shared__ __attribute__((aligned(16))) float lds[L_END];
  const int b = blockIdx.x, tid = threadIdx.x, lane = tid & 63, w = __builtin_amdgcn_readfirstlane(tid >> 6);
  const int r = lane >> 4, c = lane & 15;
  const int head = (b & 7) * 4 + (b >> 6), split = (b >> 3) & 7, P = head * 8 + split;
  const int n1 = head < 11 ? 2 : 1;
  const bool isW1 = split >= 8 - n1;
  const int g1 = split == 7 ? head : 32 + head;
  const int npw = !isW1 ? 17 : (n1 == 2 ? 13 : 9);
  const int ustart = (!isW1 ? 136 * split : (n1 == 2 ? 816 + (split - 6) * 104 : 952)) + w * npw;
  const rsrc_t rsW = mkrs(a.ws, WS_END);
  volatile LAS unsigned* flagQ = (volatile LAS unsigned*)(lds + L_FLAG);
  volatile LAS unsigned* flagR = (volatile LAS unsigned*)(lds + L_FLAG + 1);
  STAMP_DECL
  STAMP(0);

  const size_t kvbase = ((size_t)head * SK + (SK - WIN) + 4 * ustart) * DH;
  const rsrc_t rsK = mkrs(a.kc + kvbase, npw * 1024), rsV = mkrs(a.vc + kvbase, npw * 1024);
  const int rot = (head * 5 + split * 3 + w * 7) % npw;
  int soff[NL];
#pragma unroll
  for (int i = 0; i < NL; ++i) { const int t = i + rot; soff[i] = (i < npw ? (t >= npw ? t - npw : t) : i) * 1024; }
  v4u kr[NL], vr[NL], wo[2], w1[4], w2[4];
#pragma unroll
  for (int i = 0; i < 4; ++i) kr[i] = ld128(rsK, lane * 16, soff[i], AUX_NT);
  const int colq = 16 * head + 4 * (lane & 3) + r;
  if (tid == 0) { *flagQ = 0u; *flagR = 0u; }
  SB();
  bar();
  STAMP(1);

  unsigned long long tag = 0;
  if (w == 4) {
    __builtin_amdgcn_s_setprio(3);
    const v2u e = ldx64(rsW, WS_EPOCH);
    const v4u xa = *(const GAS v4u*)(a.x + 8 * lane);
    SB();
    const rsrc_t rsQ = mkrs(a.wq + head * DH + 16 * split, (D - 1) * (H * DH * 2) + 32);
    v4u wq[16];
#pragma unroll
    for (int i = 0; i < 16; ++i) wq[i] = ld128(rsQ, (lane >> 1) * (H * DH * 2) + (lane & 1) * 16, ((i + 4 * split) & 15) * 32 * (H * DH * 2), AUX_NT);
    SB();
    float xv[8];
#pragma unroll
    for (int j = 0; j < 4; ++j) { xv[2 * j] = bflo(xa[j]); xv[2 * j + 1] = bfhi(xa[j]); }
    float mean, rstd;
    ln_stats(xv, mean, rstd);
    f32x4 n0, n1v;
#pragma unroll
    for (int j = 0; j < 4; ++j) { n0[j] = rbf((xv[j] - mean) * rstd); n1v[j] = rbf((xv[4 + j] - mean) * rstd); }
    *(f32x4*)&lds[L_XS + 8 * lane] = n0;
    *(f32x4*)&lds[L_XS + 8 * lane + 4] = n1v;
    asm volatile("s_waitcnt lgkmcnt(0)" ::: "memory");
    float acc[8];
#pragma unroll
    for (int j = 0; j < 8; ++j) acc[j] = 0.f;
#pragma unroll
    for (int i = 0; i < 16; ++i) fma8(acc, wq[i], lds[L_XS + ((i + 4 * split) & 15) * 32 + (lane >> 1)]);
    f32x2 t = rows_reduce8(acc);
    t.x += dpp<XOR2>(t.x); t.x += dpp<ROR4>(t.x); t.x += dpp<ROR8>(t.x);
    t.y += dpp<XOR2>(t.y); t.y += dpp<ROR4>(t.y); t.y += dpp<ROR8>(t.y);
    tag = (((unsigned long long)e.y << 32) | e.x) + 1ull;
    const unsigned tlo = (unsigned)tag << 16, thi = (unsigned)(tag >> 16);
    const unsigned pk = cvtpk(t.x, t.y);
    if ((lane & 14) == 0) {
      const int d = 16 * split + 8 * (lane & 1) + ((r & 1) << 2) + (r & 2);
      stx128(rsW, WS_Q + (head * DH + d) * 8, v4u{tlo | (pk & 0xffffu), thi, tlo | (pk >> 16), thi});
    }
    v4u qg;
    _Pragma("unroll 1") for (unsigned n = 0;;) {
      MEMFENCE();
      qg = ldx128(rsW, WS_Q + (head * DH + 2 * lane) * 8);
      const bool ok = (qg.x & 0xffff0000u) == tlo && qg.y == thi && (qg.z & 0xffff0000u) == tlo && qg.w == thi;
      if (__all(ok)) break;
      if (spin(n)) break;
    }
    ((LAS unsigned*)(lds + L_QS))[lane] = (qg.x & 0xffffu) | (qg.z << 16);
    asm volatile("s_waitcnt lgkmcnt(0)" ::: "memory");
    if (lane == 0) *flagQ = 1u;
    __builtin_amdgcn_s_setprio(0);
  } else if (w == 5) {
    __builtin_amdgcn_s_setprio(3);
    const v2u e = ldx64(rsW, WS_EPOCH);
    int off = WS_END;
    if (lane < 32) off = WS_ACC + P * 512 + lane * 16;
    else if (lane < 48) off = WS_PART + head * 2048 + split * 256 + (lane - 32) * 16;
    else if (lane == 48) off = WS_ML + P * 16;
    else if (lane < 53) off = split == 0 ? WS_H + head * 64 + (lane - 49) * 16 : WS_END;
    const v4u sv = {SENT, SENT, SENT, SENT};
    stx128(rsW, off, sv);
    if (isW1) { stx128(rsW, WS_OP + g1 * 2048 + lane * 16, sv); stx128(rsW, WS_OP + g1 * 2048 + 1024 + lane * 16, sv); }
    asm volatile("s_waitcnt vmcnt(0)" ::: "memory");
    tag = (((unsigned long long)e.y << 32) | e.x) + 1ull;
    const unsigned tlo = (unsigned)tag, thi = (unsigned)(tag >> 32);
    if (lane == 0) stx64(rsW, WS_READY + P * 8, v2u{tlo, thi});
    _Pragma("unroll 1") for (unsigned n = 0;;) {
      MEMFENCE();
      const v4u f0 = ldx128(rsW, WS_READY + lane * 32), f1 = ldx128(rsW, WS_READY + lane * 32 + 16);
      const bool ok = f0.x == tlo && f0.y == thi && f0.z == tlo && f0.w == thi && f1.x == tlo && f1.y == thi && f1.z == tlo && f1.w == thi;
      if (__all(ok)) break;
      if (spin(n)) break;
    }
    if (lane == 0) *flagR = 1u;
    __builtin_amdgcn_s_setprio(0);
  }
  STAMP(2);
  if (isW1) {
    const rsrc_t rs1 = mkrs(a.w1, D * FF * 2), rs2 = mkrs(a.w2, FF * D * 2);
#pragma unroll
    for (int i = 0; i < 4; ++i) w1[i] = ld128(rs1, (64 * w + (lane >> 2)) * (FF * 2) + g1 * 64 + (lane & 3) * 16, i * 16 * (FF * 2), AUX_NT);
#pragma unroll
    for (int i = 0; i < 4; ++i) w2[i] = ld128(rs2, (32 * g1 + 4 * w) * (D * 2) + lane * 16, i * (D * 2), AUX_NT);
  } else {
#pragma unroll
    for (int i = 0; i < 4; ++i) { w1[i] = v4u{0u, 0u, 0u, 0u}; w2[i] = v4u{0u, 0u, 0u, 0u}; }
  }
  SB();
#pragma unroll
  for (int i = 4; i < NL; ++i) kr[i] = ld128(rsK, lane * 16, soff[i], AUX_NT);
  SB();
  STAMP(3);
#pragma unroll
  for (int i = 0; i < 8; ++i) vr[i] = ld128(rsV, lane * 16, soff[i], AUX_NT);
  SB();
  lds_wait(flagQ);
  const v4u qp = *(const LAS v4u*)(lds + L_QS + 4 * c);
  STAMP(4);
  float x[NL];
#pragma unroll
  for (int i = 8; i < NL; ++i) {
    vr[i] = ld128(rsV, lane * 16, soff[i], AUX_NT);
    SB();
#pragma unroll
    for (int k = 2 * (i - 8); k < 2 * (i - 8) + 2; ++k)
      if (k < NL) {
        float d = 0.f;
#pragma unroll
        for (int j = 0; j < 4; ++j) d = dot2(kr[k][j], qp[j], d);
        x[k] = d;
      }
    SB();
  }
  {
    const rsrc_t rsO = mkrs(a.wo, H * DH * D * 2);
#pragma unroll
    for (int i = 0; i < 2; ++i) wo[i] = ld128(rsO, (head * DH + 16 * w + 2 * (lane & 7)) * (D * 2) + split * 128 + (lane >> 3) * 16, i * (D * 2), AUX_NT);
  }
  const unsigned short xcol = __builtin_amdgcn_raw_buffer_load_b16(mkrs(a.x, D * 2), colq * 2, 0, 0);
  const unsigned short b2col = __builtin_amdgcn_raw_buffer_load_b16(mkrs(a.b2, D * 2), colq * 2, 0, 0);
  const unsigned short b1v = __builtin_amdgcn_raw_buffer_load_b16(mkrs(a.b1, FF * 2), (32 * g1 + 4 * w + (lane & 3)) * 2, 0, 0);
  SB();
  STAMP(5);
  const float x16 = row_sum(x[16]);
#define TR_STEP(HALF, CTRL, BIT) _Pragma("unroll") for (int h = 0; h < (HALF); ++h) { \
    const float lo_ = x[h], hi_ = x[h + (HALF)]; const float t1_ = lo_ + dpp<CTRL>(lo_), t2_ = hi_ + dpp<CTRL>(hi_); \
    x[h] = (c & (BIT)) ? t2_ : t1_; }
  TR_STEP(8, ROR8, 8) TR_STEP(4, XOR7, 4) TR_STEP(2, XOR2, 2) TR_STEP(1, XOR1, 1)
  const float sc = c < npw ? x[0] * C2 : -INFINITY;
  const float s16 = 16 < npw ? x16 * C2 : -INFINITY;
  const float mw = wave_max(fmaxf(sc, s16));
  const float p = __builtin_amdgcn_exp2f(sc - mw), p16 = __builtin_amdgcn_exp2f(s16 - mw);
  const float lw = wave_sum(c == 0 ? p + p16 : p);
  STAMP(6);
  float acc[8];
#pragma unroll
  for (int j = 0; j < 8; ++j) acc[j] = 0.f;
  PV<0>::run(acc, vr, p);
  fma8(acc, vr[16], p16);
  {
    const f32x2 t = rows_reduce8(acc);
    *(f32x2*)&lds[L_REC + w * 128 + 8 * c + ((r & 1) << 2) + (r & 2)] = t;
    if (lane == 0) { lds[L_LM + w] = mw; lds[L_LM + 8 + w] = lw; }
  }
  STAMP(7);
  bar();
  STAMP(8);
  if (w == 7) {
    const f32x4 m0 = *(const f32x4*)&lds[L_LM], m1 = *(const f32x4*)&lds[L_LM + 4], l0 = *(const f32x4*)&lds[L_LM + 8], l1 = *(const f32x4*)&lds[L_LM + 12];
    const float mm[8] = {m0.x, m0.y, m0.z, m0.w, m1.x, m1.y, m1.z, m1.w}, ll[8] = {l0.x, l0.y, l0.z, l0.w, l1.x, l1.y, l1.z, l1.w};
    float M = mm[0];
#pragma unroll
    for (int k = 1; k < 8; ++k) M = fmaxf(M, mm[k]);
    float L = 0.f, o0 = 0.f, o1 = 0.f;
#pragma unroll
    for (int k = 0; k < 8; ++k) {
      const float e = __builtin_amdgcn_exp2f(mm[k] - M);
      const f32x2 v = *(const f32x2*)&lds[L_REC + k * 128 + 2 * lane];
      L = fmaf(e, ll[k], L); o0 = fmaf(e, v.x, o0); o1 = fmaf(e, v.y, o1);
    }
    lds_wait(flagR);
    stx64(rsW, WS_ACC + P * 512 + lane * 8, v2u{pub(o0), pub(o1)});
    if (lane == 0) stx64(rsW, WS_ML + P * 16, v2u{pub(M), pub(L)});
    STAMP(9);
    v2u v[8], ml;
    _Pragma("unroll 1") for (unsigned n = 0;;) {
      MEMFENCE();
#pragma unroll
      for (int k = 0; k < 8; ++k) v[k] = ldx64(rsW, WS_ACC + (head * 8 + k) * 512 + lane * 8);
      ml = ldx64(rsW, WS_ML + (head * 8 + (lane & 7)) * 16);
      bool ok = ml.x != SENT && ml.y != SENT;
#pragma unroll
      for (int k = 0; k < 8; ++k) ok = ok && v[k].x != SENT && v[k].y != SENT;
      if (__all(ok)) break;
      if (spin(n)) break;
    }
    float Mh = asf(ml.x);
    Mh = fmaxf(Mh, dpp<XOR1>(Mh)); Mh = fmaxf(Mh, dpp<XOR2>(Mh)); Mh = fmaxf(Mh, dpp<XOR7>(Mh));
    const float e = __builtin_amdgcn_exp2f(asf(ml.x) - Mh);
    float Lh = e * asf(ml.y);
    Lh += dpp<XOR1>(Lh); Lh += dpp<XOR2>(Lh); Lh += dpp<XOR7>(Lh);
    const float wgt = e / Lh;
    f32x2 o = {0.f, 0.f};
#pragma unroll
    for (int k = 0; k < 8; ++k) {
      const float wk = __builtin_bit_cast(float, __builtin_amdgcn_readlane(__builtin_bit_cast(int, wgt), k));
      o.x = fmaf(wk, asf(v[k].x), o.x); o.y = fmaf(wk, asf(v[k].y), o.y);
    }
    *(f32x2*)&lds[L_OS + 2 * lane] = o;
    STAMP(10);
  }
  bar();
  STAMP(11);
  {
    const f32x2 o = *(const f32x2*)&lds[L_OS + 16 * w + 2 * (lane & 7)];
#pragma unroll
    for (int j = 0; j < 8; ++j) acc[j] = 0.f;
    fma8(acc, wo[0], o.x);
    fma8(acc, wo[1], o.y);
#define TR8(HALF, CTRL, BIT) _Pragma("unroll") for (int h = 0; h < (HALF); ++h) { \
    const float lo_ = acc[h], hi_ = acc[h + (HALF)]; const float t1_ = lo_ + dpp<CTRL>(lo_), t2_ = hi_ + dpp<CTRL>(hi_); \
    acc[h] = (lane & (BIT)) ? t2_ : t1_; }
    TR8(4, XOR7, 4) TR8(2, XOR2, 2) TR8(1, XOR1, 1)
    lds[L_RED + w * 64 + lane] = acc[0];
  }
  bar();
  STAMP(12);
  if (w == 0) {
    float s = 0.f;
#pragma unroll
    for (int k = 0; k < 8; ++k) s += lds[L_RED + k * 64 + lane];
    stx32(rsW, WS_PART + head * 2048 + split * 256 + lane * 4, pub(s));
    STAMP(13);
  }
  if (w == 1 && split == 0) {
    lds_wait(flagR);
    v4u v0, v1;
    _Pragma("unroll 1") for (unsigned n = 0;;) {
      MEMFENCE();
      v0 = ldx128(rsW, WS_PART + (lane >> 2) * 2048 + head * 64 + (lane & 3) * 16);
      v1 = ldx128(rsW, WS_PART + (16 + (lane >> 2)) * 2048 + head * 64 + (lane & 3) * 16);
      const bool ok = v0.x != SENT && v0.y != SENT && v0.z != SENT && v0.w != SENT && v1.x != SENT && v1.y != SENT && v1.z != SENT && v1.w != SENT;
      if (__all(ok)) break;
      if (spin(n)) break;
    }
    const float u0 = swap32_add(asf(v0.x) + asf(v1.x), asf(v0.z) + asf(v1.z)), u1 = swap32_add(asf(v0.y) + asf(v1.y), asf(v0.w) + asf(v1.w));
    float t = swap16_add(u0, u1);
    t += dpp<ROR8>(t); t += dpp<ROR4>(t);
    const float hv = rbf(bf2f(xcol) + rbf(t));
    if ((lane & 12) == 0) stx32(rsW, WS_H + colq * 4, pub(hv));
    STAMP(14);
  }
  if (w == 1 && split == 1) {
    lds_wait(flagR);
    v4u v[3]; unsigned hw;
    _Pragma("unroll 1") for (unsigned n = 0;;) {
      MEMFENCE();
      bool ok = true;
#pragma unroll
      for (int i = 0; i < 3; ++i) {
        const int row = 16 * i + (lane >> 2);
        v[i] = ldx128(rsW, row < NW1 ? WS_OP + row * 2048 + head * 64 + (lane & 3) * 16 : WS_END);
        ok = ok && v[i].x != SENT && v[i].y != SENT && v[i].z != SENT && v[i].w != SENT;
      }
      hw = ldx32(rsW, WS_H + colq * 4);
      ok = ok && hw != SENT;
      if (__all(ok)) break;
      if (spin(n)) break;
    }
    const float sx = asf(v[0].x) + asf(v[1].x) + asf(v[2].x), sy = asf(v[0].y) + asf(v[1].y) + asf(v[2].y), sz = asf(v[0].z) + asf(v[1].z) + asf(v[2].z),
                sw = asf(v[0].w) + asf(v[1].w) + asf(v[2].w);
    const float u0 = swap32_add(sx, sz), u1 = swap32_add(sy, sw);
    float t = swap16_add(u0, u1);
    t += dpp<ROR8>(t); t += dpp<ROR4>(t);
    const float y = rbf(rbf(t) + bf2f(b2col));
    if ((lane & 12) == 0) a.out[colq] = (unsigned short)(cvtpk(asf(hw) + y, 0.f) & 0xffffu);
    if (head == 0 && lane == 0) {
      const v2u e = ldx64(rsW, WS_EPOCH);
      const unsigned long long t2 = (((unsigned long long)e.y << 32) | e.x) + 1ull;
      stx64(rsW, WS_EPOCH, v2u{(unsigned)t2, (unsigned)(t2 >> 32)});
    }
    STAMP(18);
  }
  if (isW1) {
    if (w == 2) {
      lds_wait(flagR);
      v4u h0, h1;
      _Pragma("unroll 1") for (unsigned n = 0;;) {
        MEMFENCE();
        h0 = ldx128(rsW, WS_H + lane * 32);
        h1 = ldx128(rsW, WS_H + lane * 32 + 16);
        const bool ok = h0.x != SENT && h0.y != SENT && h0.z != SENT && h0.w != SENT && h1.x != SENT && h1.y != SENT && h1.z != SENT && h1.w != SENT;
        if (__all(ok)) break;
        if (spin(n)) break;
      }
      STAMP(15);
      const float hv[8] = {asf(h0.x), asf(h0.y), asf(h0.z), asf(h0.w), asf(h1.x), asf(h1.y), asf(h1.z), asf(h1.w)};
      float mean, rstd;
      ln_stats(hv, mean, rstd);
      f32x4 n0, n1v;
#pragma unroll
      for (int j = 0; j < 4; ++j) { n0[j] = rbf((hv[j] - mean) * rstd); n1v[j] = rbf((hv[4 + j] - mean) * rstd); }
      *(f32x4*)&lds[L_XS + 8 * lane] = n0;
      *(f32x4*)&lds[L_XS + 8 * lane + 4] = n1v;
    }
    bar();
#pragma unroll
    for (int j = 0; j < 8; ++j) acc[j] = 0.f;
#pragma unroll
    for (int i = 0; i < 4; ++i) fma8(acc, w1[i], lds[L_XS + 64 * w + 16 * i + (lane >> 2)]);
    {
      float u[4];
#pragma unroll
      for (int j = 0; j < 4; ++j) u[j] = swap32_add(acc[j], acc[j + 4]);
      float t0 = swap16_add(u[0], u[2]), t1 = swap16_add(u[1], u[3]);
      t0 += dpp<ROR8>(t0); t0 += dpp<ROR4>(t0);
      t1 += dpp<ROR8>(t1); t1 += dpp<ROR4>(t1);
      if ((lane & 12) == 0) *(f32x2*)&lds[L_UP + w * 32 + 8 * (lane & 3) + 2 * r] = f32x2{t0, t1};
    }
    bar();
    STAMP(16);
    float uu = lds[L_UP + ((lane >> 2) & 7) * 32 + 4 * w + (lane & 3)];
    uu += dpp<ROR8>(uu); uu += dpp<ROR4>(uu);
    uu = swap16_add(uu, uu);
    const float gv = rbf(gelu_tanh(rbf(rbf(uu) + bf2f(b1v))));
#pragma unroll
    for (int j = 0; j < 8; ++j) acc[j] = 0.f;
    fma8(acc, w2[0], dpp<0x00>(gv));
    fma8(acc, w2[1], dpp<0x55>(gv));
    fma8(acc, w2[2], dpp<0xAA>(gv));
    fma8(acc, w2[3], dpp<0xFF>(gv));
    *(f32x4*)&lds[L_RED2 + w * 512 + 8 * lane] = f32x4{acc[0], acc[1], acc[2], acc[3]};
    *(f32x4*)&lds[L_RED2 + w * 512 + 8 * lane + 4] = f32x4{acc[4], acc[5], acc[6], acc[7]};
    bar();
    float s = 0.f;
#pragma unroll
    for (int k = 0; k < 8; ++k) s += lds[L_RED2 + k * 512 + tid];
    stx32(rsW, WS_OP + g1 * 2048 + tid * 4, pub(s));
    STAMP(17);
  }
  STAMP_OUT(b * 8 + w);
}

extern "C" void kernel_launch(void* const* d_in, const int* in_sizes, int n_in, void* d_out, int out_size, void* d_ws,
                              size_t ws_size, hipStream_t stream) {
  Args a;
  a.x = (const unsigned short*)d_in[0];
  a.kc = (const unsigned short*)d_in[1];
  a.vc = (const unsigned short*)d_in[2];
  a.wq = (const unsigned short*)d_in[3];
  a.wo = (const unsigned short*)d_in[4];
  a.w1 = (const unsigned short*)d_in[6];
  a.b1 = (const unsigned short*)d_in[7];
  a.w2 = (const unsigned short*)d_in[8];
  a.b2 = (const unsigned short*)d_in[9];
  a.out = (unsigned short*)d_out;
  a.ws = (unsigned char*)d_ws;
  fused_block<<<256, 512, 0, stream>>>(a);
}
```

```cpp
#include <hip/hip_runtime.h>
#include <stdint.h>

#define GAS __attribute__((address_space(1)))
#define LAS __attribute__((address_space(3)))
typedef unsigned v4u __attribute__((ext_vector_type(4)));
typedef unsigned v2u __attribute__((ext_vector_type(2)));
typedef float f32x4 __attribute__((ext_vector_type(4)));
typedef float f32x2 __attribute__((ext_vector_type(2)));
typedef __bf16 bf16x2 __attribute__((ext_vector_type(2)));
typedef __amdgpu_buffer_rsrc_t rsrc_t;

constexpr int D = 512, H = 32, DH = 128, SK = 16384, WIN = 4096, FF = 1376;
constexpr int NL = 17;
constexpr int NW1 = FF / 32;
constexpr float EPS = 1e-5f;
constexpr float SCALE = 0.088388347648318440f, LOG2E = 1.4426950408889634f;
constexpr unsigned SENT = 0xFFFFFFFFu;
constexpr unsigned SPIN_CAP = 1u << 18;
constexpr int AUX_NT = 2, AUX_SC1 = 16;

constexpr int WS_EPOCH = 0;
constexpr int WS_READY = 1024;
constexpr int WS_Q = 4096;
constexpr int REC = 640;
constexpr int WS_ACC = WS_Q + 256 * 1024;
constexpr int WS_PART = WS_ACC + 256 * REC;
constexpr int WS_OP = WS_PART + 32 * 2048;
constexpr int WS_END = WS_OP + NW1 * 2048;

constexpr int L_XS = 0, L_HS = 0, L_QS = 512, L_FLAG = 576, L_LM = 592, L_REC = 640, L_GA = 1664, L_RED = 2720, L_W1 = 3232, W1S = 18,
              L_RED2 = L_W1, L_END = L_W1 + 512 * W1S;

__device__ __forceinline__ float bflo(unsigned u) { return __builtin_bit_cast(float, u << 16); }
__device__ __forceinline__ float bfhi(unsigned u) { return __builtin_bit_cast(float, u & 0xffff0000u); }
__device__ __forceinline__ float bf2f(unsigned short b) { return __builtin_bit_cast(float, (unsigned)b << 16); }
__device__ __forceinline__ unsigned cvtpk(float lo, float hi) {
  bf16x2 r = {(__bf16)lo, (__bf16)hi};
  return __builtin_bit_cast(unsigned, r);
}
__device__ __forceinline__ float rbf(float x) { return bflo(cvtpk(x, x)); }
__device__ __forceinline__ float dot2(unsigned k, unsigned q, float acc) {
  return __builtin_amdgcn_fdot2_f32_bf16(__builtin_bit_cast(bf16x2, k), __builtin_bit_cast(bf16x2, q), acc, false);
}
template <int CTRL> __device__ __forceinline__ float dpp(float x) {
  return __builtin_bit_cast(float, __builtin_amdgcn_mov_dpp(__builtin_bit_cast(int, x), CTRL, 0xf, 0xf, true));
}
constexpr int XOR1 = 0xB1, XOR2 = 0x4E, XOR7 = 0x141, ROR8 = 0x128, ROR4 = 0x124, BCAST = 0x150;
__device__ __forceinline__ float swap16_add(float a, float b) {
  auto s = __builtin_amdgcn_permlane16_swap(__float_as_uint(a), __float_as_uint(b), false, false);
  return __uint_as_float(s[0]) + __uint_as_float(s[1]);
}
__device__ __forceinline__ float swap32_add(float a, float b) {
  auto s = __builtin_amdgcn_permlane32_swap(__float_as_uint(a), __float_as_uint(b), false, false);
  return __uint_as_float(s[0]) + __uint_as_float(s[1]);
}
__device__ __forceinline__ float swap16_max(float a) {
  auto s = __builtin_amdgcn_permlane16_swap(__float_as_uint(a), __float_as_uint(a), false, false);
  return fmaxf(__uint_as_float(s[0]), __uint_as_float(s[1]));
}
__device__ __forceinline__ float swap32_max(float a) {
  auto s = __builtin_amdgcn_permlane32_swap(__float_as_uint(a), __float_as_uint(a), false, false);
  return fmaxf(__uint_as_float(s[0]), __uint_as_float(s[1]));
}
__device__ __forceinline__ float row_sum(float x) {
  x += dpp<XOR1>(x); x += dpp<XOR2>(x); x += dpp<XOR7>(x); x += dpp<ROR8>(x); return x;
}
__device__ __forceinline__ float wave_sum(float x) { x = row_sum(x); x = swap16_add(x, x); return swap32_add(x, x); }
__device__ __forceinline__ float wave_max(float x) {
  x = fmaxf(x, dpp<XOR1>(x)); x = fmaxf(x, dpp<XOR2>(x)); x = fmaxf(x, dpp<XOR7>(x)); x = fmaxf(x, dpp<ROR8>(x));
  x = swap16_max(x); return swap32_max(x);
}
__device__ __forceinline__ void fma8(float (&a)[8], v4u w, float p) {
#pragma unroll
  for (int j = 0; j < 4; ++j) { a[2 * j] = fmaf(p, bflo(w[j]), a[2 * j]); a[2 * j + 1] = fmaf(p, bfhi(w[j]), a[2 * j + 1]); }
}
template <int I> struct PV {
  static __device__ __forceinline__ void run(float (&a)[8], const v4u (&w)[NL], float p) {
    fma8(a, w[I], dpp<BCAST + I>(p));
    PV<I + 1>::run(a, w, p);
  }
};
template <> struct PV<16> { static __device__ __forceinline__ void run(float (&)[8], const v4u (&)[NL], float) {} };
__device__ __forceinline__ f32x2 rows_reduce8(const float (&a)[8]) {
  float u[4];
#pragma unroll
  for (int j = 0; j < 4; ++j) u[j] = swap16_add(a[j], a[j + 4]);
  f32x2 t;
  t.x = swap32_add(u[0], u[2]);
  t.y = swap32_add(u[1], u[3]);
  return t;
}
__device__ __forceinline__ float max3f(float a, float b, float c) { float d; asm("v_max3_f32 %0, %1, %2, %3" : "=v"(d) : "v"(a), "v"(b), "v"(c)); return d; }
__device__ __forceinline__ float max8(float x) {
  asm("s_nop 1\n\tv_max_f32_dpp %0, %0, %0 quad_perm:[1,0,3,2] row_mask:0xf bank_mask:0xf\n\ts_nop 1\n\t"
      "v_max_f32_dpp %0, %0, %0 quad_perm:[2,3,0,1] row_mask:0xf bank_mask:0xf\n\ts_nop 1\n\t"
      "v_max_f32_dpp %0, %0, %0 row_half_mirror row_mask:0xf bank_mask:0xf\n\ts_nop 1" : "+v"(x));
  return x;
}
__device__ __forceinline__ unsigned umax4(v4u v) { return __builtin_elementwise_max(__builtin_elementwise_max(v.x, v.y), __builtin_elementwise_max(v.z, v.w)); }
#define PIN(x) asm volatile("" : "+v"(x))
#define SB() __builtin_amdgcn_sched_barrier(0)
__device__ __forceinline__ void bar() {
  SB();
  asm volatile("s_waitcnt lgkmcnt(0)" ::: "memory");
  __builtin_amdgcn_s_barrier();
  asm volatile("" ::: "memory");
  SB();
}
__device__ __forceinline__ rsrc_t mkrs(const void* p, int bytes) { return __builtin_amdgcn_make_buffer_rsrc((void*)p, 0, bytes, 0x00020000); }
__device__ __forceinline__ v4u ld128(rsrc_t rs, int voff, int soff, int aux) {
  return aux == AUX_NT ? __builtin_amdgcn_raw_buffer_load_b128(rs, voff, soff, AUX_NT) : __builtin_amdgcn_raw_buffer_load_b128(rs, voff, soff, AUX_SC1);
}
__device__ __forceinline__ v4u ldx128(rsrc_t rs, int off) { return __builtin_amdgcn_raw_buffer_load_b128(rs, off, 0, AUX_SC1); }
__device__ __forceinline__ v2u ldx64(rsrc_t rs, int off) { return __builtin_amdgcn_raw_buffer_load_b64(rs, off, 0, AUX_SC1); }
__device__ __forceinline__ unsigned ldx32(rsrc_t rs, int off) { return __builtin_amdgcn_raw_buffer_load_b32(rs, off, 0, AUX_SC1); }
__device__ __forceinline__ void stx128(rsrc_t rs, int off, v4u v) { __builtin_amdgcn_raw_buffer_store_b128(v, rs, off, 0, AUX_SC1); }
__device__ __forceinline__ void stx64(rsrc_t rs, int off, v2u v) { __builtin_amdgcn_raw_buffer_store_b64(v, rs, off, 0, AUX_SC1); }
__device__ __forceinline__ void stx32(rsrc_t rs, int off, unsigned v) { __builtin_amdgcn_raw_buffer_store_b32(v, rs, off, 0, AUX_SC1); }
__device__ __forceinline__ unsigned pub(float x) { const unsigned u = __float_as_uint(x); return u < 0xFFFFFFFEu ? u : 0xFFFFFFFEu; }
__device__ __forceinline__ float asf(unsigned u) { return __uint_as_float(u); }
#define MEMFENCE() asm volatile("" ::: "memory")
__device__ __forceinline__ bool filled(unsigned m) { return __builtin_amdgcn_ballot_w64(m == SENT) == 0ull; }
__device__ __forceinline__ bool spin(unsigned& n) {
  __builtin_amdgcn_s_sleep(1);
  return ++n > SPIN_CAP;
}
#define NPRE 2
#define QPOLL0 300
#define RPOLL0 600
#define SLOWSLEEP 16
__device__ __forceinline__ bool spin_slow(unsigned& n) {
  __builtin_amdgcn_s_sleep(SLOWSLEEP);
  return ++n > SPIN_CAP;
}
#define MIDSLEEP 8
__device__ __forceinline__ bool spin_mid(unsigned& n) {
  __builtin_amdgcn_s_sleep(MIDSLEEP);
  return ++n > SPIN_CAP;
}
__device__ __forceinline__ void wait_until(unsigned long long t0, unsigned ticks) {
  _Pragma("unroll 1") for (int n = 0; n < 4096; ++n) {
    if (__builtin_amdgcn_s_memrealtime() - t0 >= ticks) break;
    __builtin_amdgcn_s_sleep(4);
  }
}
__device__ __forceinline__ void lds_wait(volatile LAS unsigned* f) {
  _Pragma("unroll 1") for (unsigned n = 0; *f == 0u;) if (spin(n)) break;
  MEMFENCE();
}

#define STAMP(i)
#define STAMP_DECL
#define STAMP_OUT(idx)

struct Args {
  const unsigned short *x, *kc, *vc, *wq, *wo, *w1, *b1, *w2, *b2;
  unsigned short* out;
  unsigned char* ws;
};

__device__ __forceinline__ v4u layer_norm8(const f32x2 (&v)[4]) {
  const float piv = __builtin_bit_cast(float, __builtin_amdgcn_readfirstlane(__builtin_bit_cast(int, v[0].x)));
  f32x2 d[4];
#pragma unroll
  for (int j = 0; j < 4; ++j) d[j] = v[j] - piv;
  const f32x2 s2 = (d[0] + d[1]) + (d[2] + d[3]);
  const f32x2 q2 = (d[0] * d[0] + d[1] * d[1]) + (d[2] * d[2] + d[3] * d[3]);
  float s = s2.x + s2.y, q = q2.x + q2.y;
  s += dpp<XOR1>(s); q += dpp<XOR1>(q);
  s += dpp<XOR2>(s); q += dpp<XOR2>(q);
  s += dpp<XOR7>(s); q += dpp<XOR7>(q);
  s += dpp<ROR8>(s); q += dpp<ROR8>(q);
  float t = swap16_add(s, q);
  t = swap32_add(t, t);
  const auto sq = __builtin_amdgcn_permlane16_swap(__float_as_uint(t), __float_as_uint(t), false, false);
  const float md = __uint_as_float(sq[0]) * (1.0f / D);
  const float var = fmaxf(fmaf(__uint_as_float(sq[1]), 1.0f / D, -md * md), 0.f);
  const float mb = rbf(piv + md);
  const float inv = __builtin_amdgcn_rcpf(rbf(__builtin_amdgcn_sqrtf(rbf(rbf(var) + EPS))));
  v4u o;
#pragma unroll
  for (int j = 0; j < 4; ++j) {
    const unsigned c = cvtpk(v[j].x - mb, v[j].y - mb);
    o[j] = cvtpk(bflo(c) * inv, bfhi(c) * inv);
  }
  return o;
}
__device__ __forceinline__ float gelu_tanh(float x) {
  constexpr float C0 = 2.0f * 0.7978845608028654f * 1.4426950408889634f, C1 = C0 * 0.044715f;
  const float e = __builtin_amdgcn_exp2f(x * fmaf(x * x, C1, C0));
  return fmaf(-x, __builtin_amdgcn_rcpf(e + 1.0f), x);
}

__global__ void __launch_bounds__(640, 3) fused_block(Args a) {
  __shared__ __attribute__((aligned(16))) float lds[L_END];
  const int b = blockIdx.x, lane = threadIdx.x & 63, wv = __builtin_amdgcn_readfirstlane(threadIdx.x >> 6);
  const int w = wv < 2 ? wv + 8 : wv - 2;
  const int r = lane >> 4, c = lane & 15;
  const int head = (b & 7) * 4 + (b >> 6), split = (b >> 3) & 7, P = head * 8 + split;
  const int n1 = head < 11 ? 2 : 1;
  const bool isW1 = split >= 8 - n1;
  const int g1 = split == 7 ? head : 32 + head;
  const int npw = !isW1 ? 17 : (n1 == 2 ? 13 : 9);
  const rsrc_t rsW = mkrs(a.ws, WS_END);
  volatile LAS unsigned* flagQ = (volatile LAS unsigned*)(lds + L_FLAG);
  LAS unsigned* ldu = (LAS unsigned*)lds;
  STAMP_DECL
  STAMP(0);
  v4u kr[NL], vr[NL];
  v2u wo[4];
  unsigned short b1v = 0, xcol = 0, b2col = 0;
  unsigned tlo = 0, thi = 0;
  float acc[8];

  if (w < 8) {
    const int ustart = (!isW1 ? 136 * split : (n1 == 2 ? 816 + (split - 6) * 104 : 952)) + w * npw;
    const size_t kvbase = ((size_t)head * SK + (SK - WIN) + 4 * ustart) * DH;
    const rsrc_t rsK = mkrs(a.kc + kvbase, npw * 1024), rsV = mkrs(a.vc + kvbase, npw * 1024);
#define ROTMODE 0
    const int rot = (ROTMODE == 0 ? head * 5 + split * 3 + w * 7 : ROTMODE == 1 ? (b >> 3) * 5 + w * 3 + (b & 7) : ROTMODE == 2 ? 0 : ROTMODE == 3 ? w * 2 + split * 5 + head * 3 : (b * 7 + w * 5)) % npw;
    int soff[NL];
#pragma unroll
    for (int i = 0; i < NL; ++i) { const int t = i + rot; soff[i] = (i < npw ? (t >= npw ? t - npw : t) : i) * 1024; }
#pragma unroll
    for (int i = 0; i < NPRE; ++i) kr[i] = ld128(rsK, lane * 16, soff[i], AUX_NT);
    if (w == 0 && lane == 0) *flagQ = 0u;
    bar();
    STAMP(1);
#pragma unroll
    for (int i = NPRE; i < 13; ++i) kr[i] = ld128(rsK, lane * 16, soff[i], AUX_NT);
    SB();
    {
      const rsrc_t rs1 = isW1 ? mkrs(a.w1, D * FF * 2) : rsK;
      const int vo = isW1 ? (64 * w + (lane >> 2)) * (FF * 2) + g1 * 64 + (lane & 3) * 16 : lane * 16;
#pragma unroll
      for (int i = 13; i < NL; ++i) kr[i] = ld128(rs1, vo, isW1 ? (i - 13) * 16 * (FF * 2) : soff[i], AUX_NT);
    }
    SB();
    STAMP(3);
#pragma unroll
    for (int i = 0; i < 13; ++i) vr[i] = ld128(rsV, lane * 16, soff[i], AUX_NT);
    SB();
    {
      const rsrc_t rs2 = isW1 ? mkrs(a.w2, FF * D * 2) : rsV;
      const int vo = isW1 ? (32 * g1 + 4 * w) * (D * 2) + lane * 16 : lane * 16;
#pragma unroll
      for (int i = 13; i < NL; ++i) vr[i] = ld128(rs2, vo, isW1 ? (i - 13) * (D * 2) : soff[i], AUX_NT);
    }
    SB();
    {
      const rsrc_t rsO = mkrs(a.wo, H * DH * D * 2);
#pragma unroll
      for (int i = 0; i < 4; ++i) wo[i] = __builtin_amdgcn_raw_buffer_load_b64(rsO, (head * DH + 16 * w + 4 * r) * (D * 2) + split * 128 + c * 8, i * (D * 2), AUX_NT);
    }
    b1v = __builtin_amdgcn_raw_buffer_load_b16(mkrs(a.b1, FF * 2), (32 * g1 + 4 * w + r) * 2, 0, 0);
    xcol = __builtin_amdgcn_raw_buffer_load_b16(mkrs(a.x, D * 2), (64 * w + 4 * c + r) * 2, 0, 0);
    SB();
    STAMP(5);
    lds_wait(flagQ);
    const v4u qp = *(const LAS v4u*)(lds + L_QS + 4 * c);
    STAMP(4);
    float x[NL];
#pragma unroll
    for (int k = 0; k < NL; ++k) {
      float d = 0.f;
#pragma unroll
      for (int j = 0; j < 4; ++j) d = dot2(kr[k][j], qp[j], d);
      x[k] = d;
    }
    const float x16 = row_sum(x[16]);
#define TR_STEP(HALF, CTRL, BIT) _Pragma("unroll") for (int h = 0; h < (HALF); ++h) { \
      const float lo_ = x[h], hi_ = x[h + (HALF)]; const float t1_ = lo_ + dpp<CTRL>(lo_), t2_ = hi_ + dpp<CTRL>(hi_); \
      x[h] = (c & (BIT)) ? t2_ : t1_; }
    TR_STEP(8, ROR8, 8) TR_STEP(4, XOR7, 4) TR_STEP(2, XOR2, 2) TR_STEP(1, XOR1, 1)
    const float sc = c < npw ? rbf(rbf(x[0]) * SCALE) * LOG2E : -INFINITY;
    const float s16 = 16 < npw ? rbf(rbf(x16) * SCALE) * LOG2E : -INFINITY;
    const float mw = wave_max(fmaxf(sc, s16));
    const float p = __builtin_amdgcn_exp2f(sc - mw), p16 = __builtin_amdgcn_exp2f(s16 - mw);
    const float lw = wave_sum(c == 0 ? p + p16 : p);
    STAMP(6);
#pragma unroll
    for (int j = 0; j < 8; ++j) acc[j] = 0.f;
    PV<0>::run(acc, vr, p);
    fma8(acc, vr[16], p16);
    const f32x2 t = rows_reduce8(acc);
    *(f32x2*)&lds[L_REC + w * 128 + 8 * c + ((r & 1) << 2) + (r & 2)] = t;
    if (lane == 0) { lds[L_LM + w] = mw; lds[L_LM + 8 + w] = lw; }
  } else if (w == 8) {
    const unsigned long long tstart = __builtin_amdgcn_s_memrealtime();
    const v2u e = ldx64(rsW, WS_EPOCH);
    const v4u xa = *(const GAS v4u*)(a.x + 8 * lane);
    const rsrc_t rsQ = mkrs(a.wq + (size_t)64 * split * (H * DH) + head * DH, 63 * (H * DH * 2) + 256);
    v4u wq[16];
#pragma unroll
    for (int i = 0; i < 16; ++i) wq[i] = ld128(rsQ, r * (H * DH * 2) + c * 16, i * 4 * (H * DH * 2), AUX_NT);
    xcol = __builtin_amdgcn_raw_buffer_load_b16(mkrs(a.x, D * 2), (16 * head + 4 * split - 8 + r) * 2, 0, 0);
    b2col = __builtin_amdgcn_raw_buffer_load_b16(mkrs(a.b2, D * 2), (16 * head + 4 * split - 8 + r) * 2, 0, 0);
    bar();
    STAMP(1);
    STAMP(3);
    f32x2 xv[4];
#pragma unroll
    for (int j = 0; j < 4; ++j) xv[j] = f32x2{bflo(xa[j]), bfhi(xa[j])};
    *(LAS v4u*)(ldu + L_XS + 4 * lane) = layer_norm8(xv);
    asm volatile("s_waitcnt lgkmcnt(0)" ::: "memory");
    STAMP(5);
#pragma unroll
    for (int j = 0; j < 8; ++j) acc[j] = 0.f;
#pragma unroll
    for (int i = 0; i < 16; ++i) {
      const unsigned xp = ldu[L_XS + 32 * split + 2 * i + (r >> 1)];
      fma8(acc, wq[i], (r & 1) ? bfhi(xp) : bflo(xp));
    }
    const f32x2 t = rows_reduce8(acc);
    const unsigned long long tag = (((unsigned long long)e.y << 32) | e.x) + 1ull;
    tlo = (unsigned)tag; thi = (unsigned)(tag >> 32);
    stx128(rsW, WS_Q + P * 1024 + (4 * c + ((r & 1) << 1) + (r >> 1)) * 16, v4u{__float_as_uint(t.x), __float_as_uint(t.y), tlo, thi});
    STAMP(19);
    wait_until(tstart, QPOLL0);
    v4u qg[8];
    _Pragma("unroll 1") for (unsigned n = 0;;) {
      MEMFENCE();
      bool ok = true;
#pragma unroll
      for (int k = 0; k < 8; ++k) qg[k] = ldx128(rsW, WS_Q + (head * 8 + k) * 1024 + lane * 16);
#pragma unroll
      for (int k = 0; k < 8; ++k) ok = ok && qg[k].z == tlo && qg[k].w == thi;
      if (__all(ok)) break;
      if (spin_slow(n)) break;
    }
    float q0 = 0.f, q1 = 0.f;
#pragma unroll
    for (int k = 0; k < 8; ++k) { q0 += asf(qg[k].x); q1 += asf(qg[k].y); }
    ldu[L_QS + lane] = cvtpk(q0, q1);
    asm volatile("s_waitcnt lgkmcnt(0)" ::: "memory");
    if (lane == 0) *flagQ = 1u;
    STAMP(2);
  } else {
    const unsigned long long tstart = __builtin_amdgcn_s_memrealtime();
    const v2u e = ldx64(rsW, WS_EPOCH);
    int off = WS_END;
    if (lane < 33) off = WS_ACC + P * REC + lane * 16;
    else if (lane < 49) off = WS_PART + head * 2048 + split * 256 + (lane - 33) * 16;
    const v4u sv = {SENT, SENT, SENT, SENT};
    stx128(rsW, off, sv);
    if (isW1) { stx128(rsW, WS_OP + g1 * 2048 + lane * 16, sv); stx128(rsW, WS_OP + g1 * 2048 + 1024 + lane * 16, sv); }
    bar();
    STAMP(1);
    STAMP(3);
    asm volatile("s_waitcnt vmcnt(0)" ::: "memory");
    STAMP(5);
    const unsigned long long tag = (((unsigned long long)e.y << 32) | e.x) + 1ull;
    tlo = (unsigned)tag; thi = (unsigned)(tag >> 32);
    if (lane == 0) stx64(rsW, WS_READY + P * 8, v2u{tlo, thi});
    STAMP(19);
    wait_until(tstart, RPOLL0);
    _Pragma("unroll 1") for (unsigned n = 0;;) {
      MEMFENCE();
      const v4u f0 = ldx128(rsW, WS_READY + lane * 32), f1 = ldx128(rsW, WS_READY + lane * 32 + 16);
      const bool ok = f0.x == tlo && f0.y == thi && f0.z == tlo && f0.w == thi && f1.x == tlo && f1.y == thi && f1.z == tlo && f1.w == thi;
      if (__all(ok)) break;
      if (spin_slow(n)) break;
    }
    STAMP(2);
  }
  STAMP(7);
  bar();
  STAMP(8);
  unsigned wpa[4], wpb[4];
  if (w == 9) {
    const f32x4 m0 = *(const f32x4*)&lds[L_LM], m1 = *(const f32x4*)&lds[L_LM + 4], l0 = *(const f32x4*)&lds[L_LM + 8], l1 = *(const f32x4*)&lds[L_LM + 12];
    f32x4 v[8];
#pragma unroll
    for (int k = 0; k < 8; ++k) v[k] = *(const f32x4*)&lds[L_REC + k * 128 + 4 * (lane & 31)];
    const float mm[8] = {m0.x, m0.y, m0.z, m0.w, m1.x, m1.y, m1.z, m1.w}, ll[8] = {l0.x, l0.y, l0.z, l0.w, l1.x, l1.y, l1.z, l1.w};
    const float M = max3f(max3f(mm[0], mm[1], mm[2]), max3f(mm[3], mm[4], mm[5]), max3f(mm[6], mm[7], mm[7]));
    float L = 0.f;
    f32x4 o = {0.f, 0.f, 0.f, 0.f};
#pragma unroll
    for (int k = 0; k < 8; ++k) {
      const float e = __builtin_amdgcn_exp2f(mm[k] - M);
      L = fmaf(e, ll[k], L);
      o += e * v[k];
    }
    if (lane == 32) o = f32x4{M, L, 0.f, 0.f};
    stx128(rsW, lane < 33 ? WS_ACC + P * REC + lane * 16 : WS_END, v4u{pub(o.x), pub(o.y), pub(o.z), pub(o.w)});
    STAMP(9);
  } else if (w < 8) {
#pragma unroll
    for (int k = 0; k < 4; ++k) {
      const unsigned sel = (k & 1) ? 0x07060302u : 0x05040100u;
      const unsigned p0 = __builtin_amdgcn_perm(wo[1][k >> 1], wo[0][k >> 1], sel), p1 = __builtin_amdgcn_perm(wo[3][k >> 1], wo[2][k >> 1], sel);
      wpa[k] = (c & 8) ? p1 : p0; wpb[k] = (c & 8) ? p0 : p1;
      PIN(wpa[k]); PIN(wpb[k]);
    }
    v4u v;
    const int off = lane < 33 ? WS_ACC + (head * 8 + w) * REC + lane * 16 : WS_END;
    _Pragma("unroll 1") for (unsigned n = 0;;) {
      MEMFENCE();
      v = ldx128(rsW, off);
      if (filled(umax4(v))) break;
      if (spin(n)) break;
    }
    if (lane < 33) *(LAS v4u*)(ldu + L_GA + w * 132 + lane * 4) = v;
    STAMP(10);
  }
  bar();
  STAMP(11);
  if (w < 8) {
    const f32x2 ml = *(const f32x2*)&lds[L_GA + (c & 7) * 132 + 128];
    const f32x2 v = *(const f32x2*)&lds[L_GA + (c & 7) * 132 + 16 * w + 4 * r + 2 * (c >> 3)];
    const float e = __builtin_amdgcn_exp2f(ml.x - max8(ml.x));
    float ox = e * v.x, oy = e * v.y, L = e * ml.y;
    ox += dpp<XOR1>(ox); oy += dpp<XOR1>(oy); L += dpp<XOR1>(L);
    ox += dpp<XOR2>(ox); oy += dpp<XOR2>(oy); L += dpp<XOR2>(L);
    ox += dpp<XOR7>(ox); oy += dpp<XOR7>(oy); L += dpp<XOR7>(L);
    const float il = __builtin_amdgcn_rcpf(L);
    const unsigned op = cvtpk(ox * il, oy * il);
    const unsigned oq = __builtin_amdgcn_mov_dpp(op, ROR8, 0xf, 0xf, true);
#pragma unroll
    for (int k = 0; k < 4; ++k) acc[k] = dot2(wpb[k], oq, dot2(wpa[k], op, 0.f));
    lds[L_RED + w * 64 + 4 * c + r] = swap16_add(swap32_add(acc[0], acc[2]), swap32_add(acc[1], acc[3]));
  }
  bar();
  STAMP(12);
  if (w == 9) {
    f32x4 v[8];
#pragma unroll
    for (int k = 0; k < 8; ++k) v[k] = *(const f32x4*)&lds[L_RED + k * 64 + 4 * c];
    const f32x4 s = ((v[0] + v[1]) + (v[2] + v[3])) + ((v[4] + v[5]) + (v[6] + v[7]));
    stx128(rsW, lane < 16 ? WS_PART + head * 2048 + split * 256 + lane * 16 : WS_END, v4u{pub(s.x), pub(s.y), pub(s.z), pub(s.w)});
    STAMP(13);
  } else if (w == 8 && split >= 2 && split < 6) {
    const int fo = 4 * head + split - 2;
    float hf, b2f = bf2f(b2col);
    {
      v4u v;
      const int off = lane < 32 ? WS_PART + lane * 2048 + fo * 16 : WS_END;
      _Pragma("unroll 1") for (unsigned n = 0;;) {
        MEMFENCE();
        v = ldx128(rsW, off);
        if (filled(umax4(v))) break;
        if (spin_mid(n)) break;
      }
      const float t = row_sum(swap16_add(swap32_add(asf(v.x), asf(v.z)), swap32_add(asf(v.y), asf(v.w))));
      hf = rbf(bf2f(xcol) + rbf(t));
      PIN(hf); PIN(b2f);
    }
    STAMP(14);
    v4u v;
    const int off = lane < NW1 ? WS_OP + lane * 2048 + fo * 16 : WS_END;
    _Pragma("unroll 1") for (unsigned n = 0;;) {
      MEMFENCE();
      v = ldx128(rsW, off);
      if (filled(umax4(v))) break;
      if (spin(n)) break;
    }
    const float t = row_sum(swap16_add(swap32_add(asf(v.x), asf(v.z)), swap32_add(asf(v.y), asf(v.w))));
    const unsigned ob = __float_as_uint(rbf(hf + rbf(rbf(t) + b2f)));
    const auto s16 = __builtin_amdgcn_permlane16_swap(ob, ob, false, false);
    const unsigned pk = __builtin_amdgcn_perm(s16[1], s16[0], 0x07060302u);
    const auto s32 = __builtin_amdgcn_permlane32_swap(pk, pk, false, false);
    if (lane == 0) {
      *(GAS v2u*)(a.out + 4 * fo) = v2u{s32[0], s32[1]};
      if (fo == 0) stx64(rsW, WS_EPOCH, v2u{tlo, thi});
    }
    STAMP(18);
  }
  if (isW1) {
    unsigned wp[4][4];
    f32x2 w2f[4][4];
    float b1f = 0.f;
    if (w < 8) {
#pragma unroll
      for (int i = 0; i < 4; ++i) {
        LAS unsigned* d = ldu + L_W1 + (64 * w + 16 * i + (lane >> 2)) * W1S + (lane & 3) * 4;
        *(LAS v2u*)d = v2u{kr[13 + i].x, kr[13 + i].y};
        *(LAS v2u*)(d + 2) = v2u{kr[13 + i].z, kr[13 + i].w};
      }
    }
    bar();
    if (w < 8) {
#pragma unroll
      for (int j = 0; j < 4; ++j) {
        const v2u A = *(const LAS v2u*)(ldu + L_W1 + (2 * lane + 128 * j) * W1S + 2 * w), B = *(const LAS v2u*)(ldu + L_W1 + (2 * lane + 128 * j + 1) * W1S + 2 * w);
        wp[j][0] = __builtin_amdgcn_perm(B.x, A.x, 0x05040100u); wp[j][1] = __builtin_amdgcn_perm(B.x, A.x, 0x07060302u);
        wp[j][2] = __builtin_amdgcn_perm(B.y, A.y, 0x05040100u); wp[j][3] = __builtin_amdgcn_perm(B.y, A.y, 0x07060302u);
#pragma unroll
        for (int k = 0; k < 4; ++k) { PIN(wp[j][k]); w2f[j][k] = f32x2{bflo(vr[13 + j][k]), bfhi(vr[13 + j][k])}; PIN(w2f[j][k]); }
      }
      b1f = bf2f(b1v);
      float xf = bf2f(xcol);
      PIN(b1f); PIN(xf);
      v4u g[8];
      const int off = WS_PART + r * 2048 + w * 256 + c * 16;
      unsigned pend = 0xFFu;
      _Pragma("unroll 1") for (unsigned n = 0;;) {
        MEMFENCE();
#pragma unroll
        for (int i = 0; i < 8; ++i) if (pend & (1u << i)) g[i] = ldx128(rsW, off + i * 8192);
#pragma unroll
        for (int i = 0; i < 8; ++i) if ((pend & (1u << i)) && filled(umax4(g[i]))) pend &= ~(1u << i);
        if (pend == 0u) break;
        if (spin(n)) break;
      }
      STAMP(15);
      f32x4 f[8];
#pragma unroll
      for (int i = 0; i < 8; ++i) f[i] = __builtin_bit_cast(f32x4, g[i]);
      const f32x4 s = ((f[0] + f[1]) + (f[2] + f[3])) + ((f[4] + f[5]) + (f[6] + f[7]));
      const float t = swap16_add(swap32_add(s.x, s.z), swap32_add(s.y, s.w));
      lds[L_HS + 64 * w + 4 * c + r] = rbf(xf + rbf(t));
    }
    bar();
    STAMP(16);
    if (w < 8) {
      f32x2 hv[4];
#pragma unroll
      for (int j = 0; j < 4; ++j) hv[j] = *(const f32x2*)&lds[L_HS + 2 * lane + 128 * j];
      const v4u xp = layer_norm8(hv);
      float u[4] = {0.f, 0.f, 0.f, 0.f};
#pragma unroll
      for (int j = 0; j < 4; ++j) {
#pragma unroll
        for (int k = 0; k < 4; ++k) u[k] = dot2(wp[j][k], xp[j], u[k]);
      }
      float uu = swap16_add(swap32_add(u[0], u[2]), swap32_add(u[1], u[3]));
      uu = row_sum(uu);
      const float gv = rbf(gelu_tanh(rbf(rbf(uu) + b1f)));
      f32x2 y[4] = {{0.f, 0.f}, {0.f, 0.f}, {0.f, 0.f}, {0.f, 0.f}};
#pragma unroll
      for (int j = 0; j < 4; ++j) {
        const float gj = __builtin_bit_cast(float, __builtin_amdgcn_readlane(__builtin_bit_cast(int, gv), 16 * j));
#pragma unroll
        for (int k = 0; k < 4; ++k) y[k] += gj * w2f[j][k];
      }
      *(f32x4*)&lds[L_RED2 + w * 512 + 8 * lane] = f32x4{y[0].x, y[0].y, y[1].x, y[1].y};
      *(f32x4*)&lds[L_RED2 + w * 512 + 8 * lane + 4] = f32x4{y[2].x, y[2].y, y[3].x, y[3].y};
    }
    bar();
    if (w < 2) {
      f32x4 v[8];
#pragma unroll
      for (int k = 0; k < 8; ++k) v[k] = *(const f32x4*)&lds[L_RED2 + k * 512 + 4 * (w * 64 + lane)];
      const f32x4 s = ((v[0] + v[1]) + (v[2] + v[3])) + ((v[4] + v[5]) + (v[6] + v[7]));
      stx128(rsW, WS_OP + g1 * 2048 + (w * 64 + lane) * 16, v4u{pub(s.x), pub(s.y), pub(s.z), pub(s.w)});
      STAMP(17);
    }
  }
  STAMP_OUT(b * 10 + w);
}

extern "C" void kernel_launch(void* const* d_in, const int* in_sizes, int n_in, void* d_out, int out_size, void* d_ws,
                              size_t ws_size, hipStream_t stream) {
  Args a;
  a.x = (const unsigned short*)d_in[0];
  a.kc = (const unsigned short*)d_in[1];
  a.vc = (const unsigned short*)d_in[2];
  a.wq = (const unsigned short*)d_in[3];
  a.wo = (const unsigned short*)d_in[4];
  a.w1 = (const unsigned short*)d_in[6];
  a.b1 = (const unsigned short*)d_in[7];
  a.w2 = (const unsigned short*)d_in[8];
  a.b2 = (const unsigned short*)d_in[9];
  a.out = (unsigned short*)d_out;
  a.ws = (unsigned char*)d_ws;
  fused_block<<<256, 640, 0, stream>>>(a);
}
```

```cpp
#include <hip/hip_runtime.h>
#include <stdint.h>

#define GAS __attribute__((address_space(1)))
#define LAS __attribute__((address_space(3)))
typedef unsigned v4u __attribute__((ext_vector_type(4)));
typedef unsigned v2u __attribute__((ext_vector_type(2)));
typedef float f32x4 __attribute__((ext_vector_type(4)));
typedef float f32x2 __attribute__((ext_vector_type(2)));
typedef __bf16 bf16x2 __attribute__((ext_vector_type(2)));
typedef __amdgpu_buffer_rsrc_t rsrc_t;

constexpr int D = 512, H = 32, DH = 128, SK = 16384, WIN = 4096, FF = 1376;
#define KW 4
#define VP 6
#define ILV 2
#define SMLATE 7
constexpr int NL = 17;
constexpr int NW1 = FF / 32;
constexpr float EPS = 1e-5f;
constexpr float SCALE = 0.088388347648318440f, LOG2E = 1.4426950408889634f;
constexpr unsigned SENT = 0xFFFFFFFFu;
constexpr unsigned SPIN_CAP = 1u << 18;
constexpr int AUX_NT = 2, AUX_SC1 = 16;

constexpr int WS_EPOCH = 0;
constexpr int WS_READY = 1024;
constexpr int WS_Q = 4096;
constexpr int REC = 640;
constexpr int WS_ACC = WS_Q + 256 * 1024;
constexpr int WS_PART = WS_ACC + 256 * REC;
constexpr int WS_OP = WS_PART + 32 * 2048;
constexpr int WS_END = WS_OP + NW1 * 2048;

constexpr int L_XS = 0, L_HS = 0, L_QS = 512, L_FLAG = 576, L_LM = 592, L_REC = 640, L_GA = 1664, L_RED = 2720, L_W1 = 3232, W1S = 18,
              L_RED2 = L_W1, L_END = L_W1 + 512 * W1S;

__device__ __forceinline__ float bflo(unsigned u) { return __builtin_bit_cast(float, u << 16); }
__device__ __forceinline__ float bfhi(unsigned u) { return __builtin_bit_cast(float, u & 0xffff0000u); }
__device__ __forceinline__ float bf2f(unsigned short b) { return __builtin_bit_cast(float, (unsigned)b << 16); }
__device__ __forceinline__ unsigned cvtpk(float lo, float hi) {
  bf16x2 r = {(__bf16)lo, (__bf16)hi};
  return __builtin_bit_cast(unsigned, r);
}
__device__ __forceinline__ float rbf(float x) { return bflo(cvtpk(x, x)); }
__device__ __forceinline__ float dot2(unsigned k, unsigned q, float acc) {
  return __builtin_amdgcn_fdot2_f32_bf16(__builtin_bit_cast(bf16x2, k), __builtin_bit_cast(bf16x2, q), acc, false);
}
template <int CTRL> __device__ __forceinline__ float dpp(float x) {
  return __builtin_bit_cast(float, __builtin_amdgcn_mov_dpp(__builtin_bit_cast(int, x), CTRL, 0xf, 0xf, true));
}
constexpr int XOR1 = 0xB1, XOR2 = 0x4E, XOR7 = 0x141, ROR8 = 0x128, ROR4 = 0x124, BCAST = 0x150;
__device__ __forceinline__ float swap16_add(float a, float b) {
  auto s = __builtin_amdgcn_permlane16_swap(__float_as_uint(a), __float_as_uint(b), false, false);
  return __uint_as_float(s[0]) + __uint_as_float(s[1]);
}
__device__ __forceinline__ float swap32_add(float a, float b) {
  auto s = __builtin_amdgcn_permlane32_swap(__float_as_uint(a), __float_as_uint(b), false, false);
  return __uint_as_float(s[0]) + __uint_as_float(s[1]);
}
__device__ __forceinline__ float swap16_max(float a) {
  auto s = __builtin_amdgcn_permlane16_swap(__float_as_uint(a), __float_as_uint(a), false, false);
  return fmaxf(__uint_as_float(s[0]), __uint_as_float(s[1]));
}
__device__ __forceinline__ float swap32_max(float a) {
  auto s = __builtin_amdgcn_permlane32_swap(__float_as_uint(a), __float_as_uint(a), false, false);
  return fmaxf(__uint_as_float(s[0]), __uint_as_float(s[1]));
}
__device__ __forceinline__ float row_sum(float x) {
  x += dpp<XOR1>(x); x += dpp<XOR2>(x); x += dpp<XOR7>(x); x += dpp<ROR8>(x); return x;
}
__device__ __forceinline__ float wave_sum(float x) { x = row_sum(x); x = swap16_add(x, x); return swap32_add(x, x); }
__device__ __forceinline__ float wave_max(float x) {
  x = fmaxf(x, dpp<XOR1>(x)); x = fmaxf(x, dpp<XOR2>(x)); x = fmaxf(x, dpp<XOR7>(x)); x = fmaxf(x, dpp<ROR8>(x));
  x = swap16_max(x); return swap32_max(x);
}
__device__ __forceinline__ void fma8(float (&a)[8], v4u w, float p) {
#pragma unroll
  for (int j = 0; j < 4; ++j) { a[2 * j] = fmaf(p, bflo(w[j]), a[2 * j]); a[2 * j + 1] = fmaf(p, bfhi(w[j]), a[2 * j + 1]); }
}
template <int N> struct IC { static constexpr int value = N; };
template <int I> struct PV {
  template <class F> static __device__ __forceinline__ void run(float (&a)[8], const v4u (&w)[NL], float p, float p16, F&& issue) {
    fma8(a, w[I], I < 16 ? dpp<BCAST + (I & 15)>(p) : p16);
    asm volatile("" : "+v"(a[0]), "+v"(a[1]), "+v"(a[2]), "+v"(a[3]), "+v"(a[4]), "+v"(a[5]), "+v"(a[6]), "+v"(a[7]));
    issue(IC<I + VP>{});
    PV<I + 1>::run(a, w, p, p16, issue);
  }
};
template <> struct PV<NL> { template <class F> static __device__ __forceinline__ void run(float (&)[8], const v4u (&)[NL], float, float, F&&) {} };
__device__ __forceinline__ f32x2 rows_reduce8(const float (&a)[8]) {
  float u[4];
#pragma unroll
  for (int j = 0; j < 4; ++j) u[j] = swap16_add(a[j], a[j + 4]);
  f32x2 t;
  t.x = swap32_add(u[0], u[2]);
  t.y = swap32_add(u[1], u[3]);
  return t;
}
__device__ __forceinline__ float max3f(float a, float b, float c) { float d; asm("v_max3_f32 %0, %1, %2, %3" : "=v"(d) : "v"(a), "v"(b), "v"(c)); return d; }
__device__ __forceinline__ float max8(float x) {
  asm("s_nop 1\n\tv_max_f32_dpp %0, %0, %0 quad_perm:[1,0,3,2] row_mask:0xf bank_mask:0xf\n\ts_nop 1\n\t"
      "v_max_f32_dpp %0, %0, %0 quad_perm:[2,3,0,1] row_mask:0xf bank_mask:0xf\n\ts_nop 1\n\t"
      "v_max_f32_dpp %0, %0, %0 row_half_mirror row_mask:0xf bank_mask:0xf\n\ts_nop 1" : "+v"(x));
  return x;
}
__device__ __forceinline__ unsigned umax4(v4u v) { return __builtin_elementwise_max(__builtin_elementwise_max(v.x, v.y), __builtin_elementwise_max(v.z, v.w)); }
#define PIN(x) asm volatile("" : "+v"(x))
#define SB() __builtin_amdgcn_sched_barrier(0)
__device__ __forceinline__ void bar() {
  SB();
  asm volatile("s_waitcnt lgkmcnt(0)" ::: "memory");
  __builtin_amdgcn_s_barrier();
  asm volatile("" ::: "memory");
  SB();
}
__device__ __forceinline__ rsrc_t mkrs(const void* p, int bytes) { return __builtin_amdgcn_make_buffer_rsrc((void*)p, 0, bytes, 0x00020000); }
__device__ __forceinline__ v4u ld128(rsrc_t rs, int voff, int soff, int aux) {
  return aux == AUX_NT ? __builtin_amdgcn_raw_buffer_load_b128(rs, voff, soff, AUX_NT) : __builtin_amdgcn_raw_buffer_load_b128(rs, voff, soff, AUX_SC1);
}
__device__ __forceinline__ v4u ldx128(rsrc_t rs, int off) { return __builtin_amdgcn_raw_buffer_load_b128(rs, off, 0, AUX_SC1); }
__device__ __forceinline__ v2u ldx64(rsrc_t rs, int off) { return __builtin_amdgcn_raw_buffer_load_b64(rs, off, 0, AUX_SC1); }
__device__ __forceinline__ unsigned ldx32(rsrc_t rs, int off) { return __builtin_amdgcn_raw_buffer_load_b32(rs, off, 0, AUX_SC1); }
__device__ __forceinline__ void stx128(rsrc_t rs, int off, v4u v) { __builtin_amdgcn_raw_buffer_store_b128(v, rs, off, 0, AUX_SC1); }
__device__ __forceinline__ void stx64(rsrc_t rs, int off, v2u v) { __builtin_amdgcn_raw_buffer_store_b64(v, rs, off, 0, AUX_SC1); }
__device__ __forceinline__ void stx32(rsrc_t rs, int off, unsigned v) { __builtin_amdgcn_raw_buffer_store_b32(v, rs, off, 0, AUX_SC1); }
__device__ __forceinline__ unsigned pub(float x) { const unsigned u = __float_as_uint(x); return u < 0xFFFFFFFEu ? u : 0xFFFFFFFEu; }
__device__ __forceinline__ float asf(unsigned u) { return __uint_as_float(u); }
#define MEMFENCE() asm volatile("" ::: "memory")
__device__ __forceinline__ bool filled(unsigned m) { return __builtin_amdgcn_ballot_w64(m == SENT) == 0ull; }
#define SPINSLEEP 1
#define SPINSLEEP_B 1
template <int S = 1> __device__ __forceinline__ bool spin(unsigned& n) {
  __builtin_amdgcn_s_sleep(S);
  return ++n > SPIN_CAP;
}
__device__ __forceinline__ v4u poll_slot(rsrc_t rs, int off) {
  v4u v;
  _Pragma("unroll 1") for (unsigned n = 0;;) {
    MEMFENCE();
    v = ldx128(rs, off);
    if (filled(umax4(v))) break;
    if (spin<SPINSLEEP>(n)) break;
  }
  return v;
}
#define QPOLL0 300
#define RPOLL0 600
#define SLOWSLEEP 16
__device__ __forceinline__ bool spin_slow(unsigned& n) {
  __builtin_amdgcn_s_sleep(SLOWSLEEP);
  return ++n > SPIN_CAP;
}
#define MIDSLEEP 8
__device__ __forceinline__ bool spin_mid(unsigned& n) {
  __builtin_amdgcn_s_sleep(MIDSLEEP);
  return ++n > SPIN_CAP;
}
__device__ __forceinline__ void wait_until(unsigned long long t0, unsigned ticks) {
  _Pragma("unroll 1") for (int n = 0; n < 4096; ++n) {
    if (__builtin_amdgcn_s_memrealtime() - t0 >= ticks) break;
    __builtin_amdgcn_s_sleep(4);
  }
}
#define PACE 510
#define PT0 60
__device__ __forceinline__ void gate(unsigned long long t0, unsigned ticks) {
  SB();
  _Pragma("unroll 1") for (int n = 0; n < 4096; ++n) {
    if ((unsigned)(__builtin_amdgcn_s_memrealtime() - t0) >= ticks) break;
    __builtin_amdgcn_s_sleep(1);
  }
  MEMFENCE();
  SB();
}
__device__ __forceinline__ void lds_wait(volatile LAS unsigned* f) {
  _Pragma("unroll 1") for (unsigned n = 0; *f == 0u;) if (spin(n)) break;
  MEMFENCE();
}

struct Args {
  const unsigned short *x, *kc, *vc, *wq, *wo, *w1, *b1, *w2, *b2;
  unsigned short* out;
  unsigned char* ws;
};

__device__ __forceinline__ v4u layer_norm8(const f32x2 (&v)[4]) {
  const float piv = __builtin_bit_cast(float, __builtin_amdgcn_readfirstlane(__builtin_bit_cast(int, v[0].x)));
  f32x2 d[4];
#pragma unroll
  for (int j = 0; j < 4; ++j) d[j] = v[j] - piv;
  const f32x2 s2 = (d[0] + d[1]) + (d[2] + d[3]);
  const f32x2 q2 = (d[0] * d[0] + d[1] * d[1]) + (d[2] * d[2] + d[3] * d[3]);
  float s = s2.x + s2.y, q = q2.x + q2.y;
  s += dpp<XOR1>(s); q += dpp<XOR1>(q);
  s += dpp<XOR2>(s); q += dpp<XOR2>(q);
  s += dpp<XOR7>(s); q += dpp<XOR7>(q);
  s += dpp<ROR8>(s); q += dpp<ROR8>(q);
  float t = swap16_add(s, q);
  t = swap32_add(t, t);
  const auto sq = __builtin_amdgcn_permlane16_swap(__float_as_uint(t), __float_as_uint(t), false, false);
  const float md = __uint_as_float(sq[0]) * (1.0f / D);
  const float var = fmaxf(fmaf(__uint_as_float(sq[1]), 1.0f / D, -md * md), 0.f);
  const float mb = rbf(piv + md);
  const float inv = __builtin_amdgcn_rcpf(rbf(__builtin_amdgcn_sqrtf(rbf(rbf(var) + EPS))));
  v4u o;
#pragma unroll
  for (int j = 0; j < 4; ++j) {
    const unsigned c = cvtpk(v[j].x - mb, v[j].y - mb);
    o[j] = cvtpk(bflo(c) * inv, bfhi(c) * inv);
  }
  return o;
}
__device__ __forceinline__ float gelu_tanh(float x) {
  constexpr float C0 = 2.0f * 0.7978845608028654f * 1.4426950408889634f, C1 = C0 * 0.044715f;
  const float e = __builtin_amdgcn_exp2f(x * fmaf(x * x, C1, C0));
  return fmaf(-x, __builtin_amdgcn_rcpf(e + 1.0f), x);
}

__global__ void __launch_bounds__(640, 3) fused_block(Args a) {
  __shared__ __attribute__((aligned(16))) float lds[L_END];
  const int b = blockIdx.x, lane = threadIdx.x & 63, wv = __builtin_amdgcn_readfirstlane(threadIdx.x >> 6);
  const int w = wv < 2 ? wv + 8 : wv - 2;
  const int r = lane >> 4, c = lane & 15;
  const int head = (b & 7) * 4 + (b >> 6), split = (b >> 3) & 7, P = head * 8 + split;
  const int n1 = head < 11 ? 2 : 1;
  const bool isW1 = split >= 8 - n1;
  const int g1 = split == 7 ? head : 32 + head;
  const int npw = !isW1 ? 17 : (n1 == 2 ? 13 : 9);
  const rsrc_t rsW = mkrs(a.ws, WS_END);
  volatile LAS unsigned* flagQ = (volatile LAS unsigned*)(lds + L_FLAG);
  LAS unsigned* ldu = (LAS unsigned*)lds;
  v4u kr[NL], vr[NL];
  v2u wo[4];
  unsigned short b1v = 0, xcol = 0, b2col = 0;
  unsigned tlo = 0, thi = 0;
  float acc[8];

  if (w < 8) {
    const unsigned long long tstart = __builtin_amdgcn_s_memrealtime();
#define GATE(i) do { if (PACE) gate(tstart, (PT0 * 16 + (i) * PACE + w * (PACE / 8)) >> 4); } while (0)
    const int ustart = (!isW1 ? 136 * split : (n1 == 2 ? 816 + (split - 6) * 104 : 952)) + w * npw;
    const size_t kvbase = ((size_t)head * SK + (SK - WIN) + 4 * ustart) * DH;
    const rsrc_t rsK = mkrs(a.kc + kvbase, npw * 1024), rsV = mkrs(a.vc + kvbase, npw * 1024);
#define ROTMODE 0
    const int rot = (ROTMODE == 0 ? head * 5 + split * 3 + w * 7 : ROTMODE == 1 ? (b >> 3) * 5 + w * 3 + (b & 7) : ROTMODE == 2 ? 0 : ROTMODE == 3 ? w * 2 + split * 5 + head * 3 : (b * 7 + w * 5)) % npw;
    int soff[NL];
#pragma unroll
    for (int i = 0; i < NL; ++i) { const int t = i + rot; soff[i] = (i < npw ? (t >= npw ? t - npw : t) : i) * 1024; }
    const rsrc_t rs1 = isW1 ? mkrs(a.w1, D * FF * 2) : rsK;
    const int vo1 = isW1 ? (64 * w + (lane >> 2)) * (FF * 2) + g1 * 64 + (lane & 3) * 16 : lane * 16;
    const rsrc_t rs2 = isW1 ? mkrs(a.w2, FF * D * 2) : rsV;
    const int vo2 = isW1 ? (32 * g1 + 4 * w) * (D * 2) + lane * 16 : lane * 16;
#define LOADK(i) kr[i] = (i) < 13 ? ld128(rsK, lane * 16, soff[i], AUX_NT) : ld128(rs1, vo1, isW1 ? ((i) - 13) * 16 * (FF * 2) : soff[i], AUX_NT)
#define LOADV(i) vr[i] = (i) < 13 ? ld128(rsV, lane * 16, soff[i], AUX_NT) : ld128(rs2, vo2, isW1 ? ((i) - 13) * (D * 2) : soff[i], AUX_NT)
#define WINDOW(n) do { SB(); asm volatile("s_waitcnt vmcnt(%0)" :: "n"((n) - 1) : "memory"); SB(); } while (0)
#define LOADWO(i) wo[i] = __builtin_amdgcn_raw_buffer_load_b64(mkrs(a.wo, H * DH * D * 2), (head * DH + 16 * w + 4 * r) * (D * 2) + split * 128 + c * 8, (i) * (D * 2), AUX_NT)
#define LOADB1X() do { b1v = __builtin_amdgcn_raw_buffer_load_b16(mkrs(a.b1, FF * 2), (32 * g1 + 4 * w + r) * 2, 0, 0); \
      xcol = __builtin_amdgcn_raw_buffer_load_b16(mkrs(a.x, D * 2), (64 * w + 4 * c + r) * 2, 0, 0); } while (0)
    LOADK(0);
    LOADK(1);
    if (w == 0 && lane == 0) *flagQ = 0u;
    bar();
#pragma unroll
    for (int i = 2; i < NL; ++i) { if (i >= KW) WINDOW(KW); GATE(i); LOADK(i); }
    float x[NL];
#pragma unroll
    for (int i = 0; i < ILV; ++i) { WINDOW(KW); GATE(NL + i); LOADV(i); }
    SB();
    lds_wait(flagQ);
    const v4u qp = *(const LAS v4u*)(lds + L_QS + 4 * c);
#define QK(k) do { float d_ = 0.f; _Pragma("unroll") for (int j = 0; j < 4; ++j) d_ = dot2(kr[k][j], qp[j], d_); x[k] = d_; PIN(x[k]); } while (0)
#pragma unroll
    for (int k = 0; k < NL - KW + ILV; ++k) QK(k);
#pragma unroll
    for (int i = ILV; i < VP; ++i) {
      if (i < KW) { WINDOW(KW); GATE(NL + i); }
      SB(); LOADV(i); SB();
      if (NL - KW + i < NL) QK(NL - KW + i);
    }
    SB();
    const float x16 = row_sum(x[16]);
#define TR_STEP(HALF, CTRL, BIT) _Pragma("unroll") for (int h = 0; h < (HALF); ++h) { \
      const float lo_ = x[h], hi_ = x[h + (HALF)]; const float t1_ = lo_ + dpp<CTRL>(lo_), t2_ = hi_ + dpp<CTRL>(hi_); \
      x[h] = (c & (BIT)) ? t2_ : t1_; }
    TR_STEP(8, ROR8, 8) TR_STEP(4, XOR7, 4) TR_STEP(2, XOR2, 2) TR_STEP(1, XOR1, 1)
    const float sc = c < npw ? rbf(rbf(x[0]) * SCALE) * LOG2E : -INFINITY;
    const float s16 = 16 < npw ? rbf(rbf(x16) * SCALE) * LOG2E : -INFINITY;
    const float mw = wave_max(fmaxf(sc, s16));
    const float p = __builtin_amdgcn_exp2f(sc - mw), p16 = __builtin_amdgcn_exp2f(s16 - mw);
    const float lw = wave_sum(c == 0 ? p + p16 : p);
#pragma unroll
    for (int j = 0; j < 8; ++j) acc[j] = 0.f;
    PV<0>::run(acc, vr, p, p16, [&](auto ic) __attribute__((always_inline)) { constexpr int i = decltype(ic)::value; if constexpr (i < NL) { SB(); GATE(NL + i); LOADV(i);
        if constexpr (i >= SMLATE && i < SMLATE + 4) LOADWO(i - SMLATE);
        if constexpr (i == SMLATE + 4) LOADB1X();
        SB(); } });
    const f32x2 t = rows_reduce8(acc);
    *(f32x2*)&lds[L_REC + w * 128 + 8 * c + ((r & 1) << 2) + (r & 2)] = t;
    if (lane == 0) { lds[L_LM + w] = mw; lds[L_LM + 8 + w] = lw; }
  } else if (w == 8) {
    const unsigned long long tstart = __builtin_amdgcn_s_memrealtime();
    const v2u e = ldx64(rsW, WS_EPOCH);
    const v4u xa = *(const GAS v4u*)(a.x + 8 * lane);
    const rsrc_t rsQ = mkrs(a.wq + (size_t)64 * split * (H * DH) + head * DH, 63 * (H * DH * 2) + 256);
    v4u wq[16];
#pragma unroll
    for (int i = 0; i < 16; ++i) wq[i] = ld128(rsQ, r * (H * DH * 2) + c * 16, i * 4 * (H * DH * 2), AUX_NT);
    xcol = __builtin_amdgcn_raw_buffer_load_b16(mkrs(a.x, D * 2), (16 * head + 4 * split - 8 + r) * 2, 0, 0);
    b2col = __builtin_amdgcn_raw_buffer_load_b16(mkrs(a.b2, D * 2), (16 * head + 4 * split - 8 + r) * 2, 0, 0);
    bar();
    f32x2 xv[4];
#pragma unroll
    for (int j = 0; j < 4; ++j) xv[j] = f32x2{bflo(xa[j]), bfhi(xa[j])};
    *(LAS v4u*)(ldu + L_XS + 4 * lane) = layer_norm8(xv);
    asm volatile("s_waitcnt lgkmcnt(0)" ::: "memory");
#pragma unroll
    for (int j = 0; j < 8; ++j) acc[j] = 0.f;
#pragma unroll
    for (int i = 0; i < 16; ++i) {
      const unsigned xp = ldu[L_XS + 32 * split + 2 * i + (r >> 1)];
      fma8(acc, wq[i], (r & 1) ? bfhi(xp) : bflo(xp));
    }
    const f32x2 t = rows_reduce8(acc);
    const unsigned long long tag = (((unsigned long long)e.y << 32) | e.x) + 1ull;
    tlo = (unsigned)tag; thi = (unsigned)(tag >> 32);
    stx128(rsW, WS_Q + P * 1024 + (4 * c + ((r & 1) << 1) + (r >> 1)) * 16, v4u{__float_as_uint(t.x), __float_as_uint(t.y), tlo, thi});
    wait_until(tstart, QPOLL0);
    v4u qg[8];
    _Pragma("unroll 1") for (unsigned n = 0;;) {
      MEMFENCE();
      bool ok = true;
#pragma unroll
      for (int k = 0; k < 8; ++k) qg[k] = ldx128(rsW, WS_Q + (head * 8 + k) * 1024 + lane * 16);
#pragma unroll
      for (int k = 0; k < 8; ++k) ok = ok && qg[k].z == tlo && qg[k].w == thi;
      if (__all(ok)) break;
      if (spin_slow(n)) break;
    }
    float q0 = 0.f, q1 = 0.f;
#pragma unroll
    for (int k = 0; k < 8; ++k) { q0 += asf(qg[k].x); q1 += asf(qg[k].y); }
    ldu[L_QS + lane] = cvtpk(q0, q1);
    asm volatile("s_waitcnt lgkmcnt(0)" ::: "memory");
    if (lane == 0) *flagQ = 1u;
  } else {
    const unsigned long long tstart = __builtin_amdgcn_s_memrealtime();
    const v2u e = ldx64(rsW, WS_EPOCH);
    int off = WS_END;
    if (lane < 33) off = WS_ACC + P * REC + lane * 16;
    else if (lane < 49) off = WS_PART + head * 2048 + split * 256 + (lane - 33) * 16;
    const v4u sv = {SENT, SENT, SENT, SENT};
    stx128(rsW, off, sv);
    if (isW1) { stx128(rsW, WS_OP + g1 * 2048 + lane * 16, sv); stx128(rsW, WS_OP + g1 * 2048 + 1024 + lane * 16, sv); }
    bar();
    asm volatile("s_waitcnt vmcnt(0)" ::: "memory");
    const unsigned long long tag = (((unsigned long long)e.y << 32) | e.x) + 1ull;
    tlo = (unsigned)tag; thi = (unsigned)(tag >> 32);
    if (lane == 0) stx64(rsW, WS_READY + P * 8, v2u{tlo, thi});
    wait_until(tstart, RPOLL0);
    _Pragma("unroll 1") for (unsigned n = 0;;) {
      MEMFENCE();
      const v4u f0 = ldx128(rsW, WS_READY + lane * 32), f1 = ldx128(rsW, WS_READY + lane * 32 + 16);
      const bool ok = f0.x == tlo && f0.y == thi && f0.z == tlo && f0.w == thi && f1.x == tlo && f1.y == thi && f1.z == tlo && f1.w == thi;
      if (__all(ok)) break;
      if (spin_slow(n)) break;
    }
  }
  bar();
  unsigned wpa[4], wpb[4];
  if (w == 9) {
    const f32x4 m0 = *(const f32x4*)&lds[L_LM], m1 = *(const f32x4*)&lds[L_LM + 4], l0 = *(const f32x4*)&lds[L_LM + 8], l1 = *(const f32x4*)&lds[L_LM + 12];
    f32x4 v[8];
#pragma unroll
    for (int k = 0; k < 8; ++k) v[k] = *(const f32x4*)&lds[L_REC + k * 128 + 4 * (lane & 31)];
    const float mm[8] = {m0.x, m0.y, m0.z, m0.w, m1.x, m1.y, m1.z, m1.w}, ll[8] = {l0.x, l0.y, l0.z, l0.w, l1.x, l1.y, l1.z, l1.w};
    const float M = max3f(max3f(mm[0], mm[1], mm[2]), max3f(mm[3], mm[4], mm[5]), max3f(mm[6], mm[7], mm[7]));
    float L = 0.f;
    f32x4 o = {0.f, 0.f, 0.f, 0.f};
#pragma unroll
    for (int k = 0; k < 8; ++k) {
      const float e = __builtin_amdgcn_exp2f(mm[k] - M);
      L = fmaf(e, ll[k], L);
      o += e * v[k];
    }
    if (lane == 32) o = f32x4{M, L, 0.f, 0.f};
    stx128(rsW, lane < 33 ? WS_ACC + P * REC + lane * 16 : WS_END, v4u{pub(o.x), pub(o.y), pub(o.z), pub(o.w)});
  } else if (w < 8) {
#pragma unroll
    for (int k = 0; k < 4; ++k) {
      const unsigned sel = (k & 1) ? 0x07060302u : 0x05040100u;
      const unsigned p0 = __builtin_amdgcn_perm(wo[1][k >> 1], wo[0][k >> 1], sel), p1 = __builtin_amdgcn_perm(wo[3][k >> 1], wo[2][k >> 1], sel);
      wpa[k] = (c & 8) ? p1 : p0; wpb[k] = (c & 8) ? p0 : p1;
      PIN(wpa[k]); PIN(wpb[k]);
    }
    const v4u v = poll_slot(rsW, lane < 33 ? WS_ACC + (head * 8 + w) * REC + lane * 16 : WS_END);
    if (lane < 33) *(LAS v4u*)(ldu + L_GA + w * 132 + lane * 4) = v;
  }
  bar();
  if (w < 8) {
    const f32x2 ml = *(const f32x2*)&lds[L_GA + (c & 7) * 132 + 128];
    const f32x2 v = *(const f32x2*)&lds[L_GA + (c & 7) * 132 + 16 * w + 4 * r + 2 * (c >> 3)];
    const float e = __builtin_amdgcn_exp2f(ml.x - max8(ml.x));
    float ox = e * v.x, oy = e * v.y, L = e * ml.y;
    ox += dpp<XOR1>(ox); oy += dpp<XOR1>(oy); L += dpp<XOR1>(L);
    ox += dpp<XOR2>(ox); oy += dpp<XOR2>(oy); L += dpp<XOR2>(L);
    ox += dpp<XOR7>(ox); oy += dpp<XOR7>(oy); L += dpp<XOR7>(L);
    const float il = __builtin_amdgcn_rcpf(L);
    const unsigned op = cvtpk(ox * il, oy * il);
    const unsigned oq = __builtin_amdgcn_mov_dpp(op, ROR8, 0xf, 0xf, true);
#pragma unroll
    for (int k = 0; k < 4; ++k) acc[k] = dot2(wpb[k], oq, dot2(wpa[k], op, 0.f));
    lds[L_RED + w * 64 + 4 * c + r] = swap16_add(swap32_add(acc[0], acc[2]), swap32_add(acc[1], acc[3]));
  }
  bar();
  if (w == 9) {
    f32x4 v[8];
#pragma unroll
    for (int k = 0; k < 8; ++k) v[k] = *(const f32x4*)&lds[L_RED + k * 64 + 4 * c];
    const f32x4 s = ((v[0] + v[1]) + (v[2] + v[3])) + ((v[4] + v[5]) + (v[6] + v[7]));
    stx128(rsW, lane < 16 ? WS_PART + head * 2048 + split * 256 + lane * 16 : WS_END, v4u{pub(s.x), pub(s.y), pub(s.z), pub(s.w)});
  } else if (w == 8 && split >= 2 && split < 6) {
    const int fo = 4 * head + split - 2;
    float hf, b2f = bf2f(b2col);
    {
      v4u v;
      const int off = lane < 32 ? WS_PART + lane * 2048 + fo * 16 : WS_END;
      _Pragma("unroll 1") for (unsigned n = 0;;) {
        MEMFENCE();
        v = ldx128(rsW, off);
        if (filled(umax4(v))) break;
        if (spin_mid(n)) break;
      }
      const float t = row_sum(swap16_add(swap32_add(asf(v.x), asf(v.z)), swap32_add(asf(v.y), asf(v.w))));
      hf = rbf(bf2f(xcol) + rbf(t));
      PIN(hf); PIN(b2f);
    }
    const v4u v = poll_slot(rsW, lane < NW1 ? WS_OP + lane * 2048 + fo * 16 : WS_END);
    const float t = row_sum(swap16_add(swap32_add(asf(v.x), asf(v.z)), swap32_add(asf(v.y), asf(v.w))));
    const unsigned ob = __float_as_uint(rbf(hf + rbf(rbf(t) + b2f)));
    const auto s16 = __builtin_amdgcn_permlane16_swap(ob, ob, false, false);
    const unsigned pk = __builtin_amdgcn_perm(s16[1], s16[0], 0x07060302u);
    const auto s32 = __builtin_amdgcn_permlane32_swap(pk, pk, false, false);
    if (lane == 0) {
      *(GAS v2u*)(a.out + 4 * fo) = v2u{s32[0], s32[1]};
      if (fo == 0) stx64(rsW, WS_EPOCH, v2u{tlo, thi});
    }
  }
  if (isW1) {
    unsigned wp[4][4];
    f32x2 w2f[4][4];
    float b1f = 0.f;
    if (w < 8) {
#pragma unroll
      for (int i = 0; i < 4; ++i) {
        LAS unsigned* d = ldu + L_W1 + (64 * w + 16 * i + (lane >> 2)) * W1S + (lane & 3) * 4;
        *(LAS v2u*)d = v2u{kr[13 + i].x, kr[13 + i].y};
        *(LAS v2u*)(d + 2) = v2u{kr[13 + i].z, kr[13 + i].w};
      }
    }
    bar();
    if (w < 8) {
#pragma unroll
      for (int j = 0; j < 4; ++j) {
        const v2u A = *(const LAS v2u*)(ldu + L_W1 + (2 * lane + 128 * j) * W1S + 2 * w), B = *(const LAS v2u*)(ldu + L_W1 + (2 * lane + 128 * j + 1) * W1S + 2 * w);
        wp[j][0] = __builtin_amdgcn_perm(B.x, A.x, 0x05040100u); wp[j][1] = __builtin_amdgcn_perm(B.x, A.x, 0x07060302u);
        wp[j][2] = __builtin_amdgcn_perm(B.y, A.y, 0x05040100u); wp[j][3] = __builtin_amdgcn_perm(B.y, A.y, 0x07060302u);
#pragma unroll
        for (int k = 0; k < 4; ++k) { PIN(wp[j][k]); w2f[j][k] = f32x2{bflo(vr[13 + j][k]), bfhi(vr[13 + j][k])}; PIN(w2f[j][k]); }
      }
      b1f = bf2f(b1v);
      float xf = bf2f(xcol);
      PIN(b1f); PIN(xf);
      v4u g[8];
      const int off = WS_PART + r * 2048 + w * 256 + c * 16;
      unsigned pend = 0xFFu;
      _Pragma("unroll 1") for (unsigned n = 0;;) {
        MEMFENCE();
#pragma unroll
        for (int i = 0; i < 8; ++i) if (pend & (1u << i)) g[i] = ldx128(rsW, off + i * 8192);
#pragma unroll
        for (int i = 0; i < 8; ++i) if ((pend & (1u << i)) && filled(umax4(g[i]))) pend &= ~(1u << i);
        if (pend == 0u) break;
        if (spin<SPINSLEEP_B>(n)) break;
      }
      f32x4 f[8];
#pragma unroll
      for (int i = 0; i < 8; ++i) f[i] = __builtin_bit_cast(f32x4, g[i]);
      const f32x4 s = ((f[0] + f[1]) + (f[2] + f[3])) + ((f[4] + f[5]) + (f[6] + f[7]));
      const float t = swap16_add(swap32_add(s.x, s.z), swap32_add(s.y, s.w));
      lds[L_HS + 64 * w + 4 * c + r] = rbf(xf + rbf(t));
    }
    bar();
    if (w < 8) {
      f32x2 hv[4];
#pragma unroll
      for (int j = 0; j < 4; ++j) hv[j] = *(const f32x2*)&lds[L_HS + 2 * lane + 128 * j];
      const v4u xp = layer_norm8(hv);
      float u[4] = {0.f, 0.f, 0.f, 0.f};
#pragma unroll
      for (int j = 0; j < 4; ++j) {
#pragma unroll
        for (int k = 0; k < 4; ++k) u[k] = dot2(wp[j][k], xp[j], u[k]);
      }
      float uu = swap16_add(swap32_add(u[0], u[2]), swap32_add(u[1], u[3]));
      uu = row_sum(uu);
      const float gv = rbf(gelu_tanh(rbf(rbf(uu) + b1f)));
      f32x2 y[4] = {{0.f, 0.f}, {0.f, 0.f}, {0.f, 0.f}, {0.f, 0.f}};
#pragma unroll
      for (int j = 0; j < 4; ++j) {
        const float gj = __builtin_bit_cast(float, __builtin_amdgcn_readlane(__builtin_bit_cast(int, gv), 16 * j));
#pragma unroll
        for (int k = 0; k < 4; ++k) y[k] += gj * w2f[j][k];
      }
      *(f32x4*)&lds[L_RED2 + w * 512 + 8 * lane] = f32x4{y[0].x, y[0].y, y[1].x, y[1].y};
      *(f32x4*)&lds[L_RED2 + w * 512 + 8 * lane + 4] = f32x4{y[2].x, y[2].y, y[3].x, y[3].y};
    }
    bar();
    if (w < 2) {
      f32x4 v[8];
#pragma unroll
      for (int k = 0; k < 8; ++k) v[k] = *(const f32x4*)&lds[L_RED2 + k * 512 + 4 * (w * 64 + lane)];
      const f32x4 s = ((v[0] + v[1]) + (v[2] + v[3])) + ((v[4] + v[5]) + (v[6] + v[7]));
      stx128(rsW, WS_OP + g1 * 2048 + (w * 64 + lane) * 16, v4u{pub(s.x), pub(s.y), pub(s.z), pub(s.w)});
    }
  }
}

extern "C" void kernel_launch(void* const* d_in, const int* in_sizes, int n_in, void* d_out, int out_size, void* d_ws,
                              size_t ws_size, hipStream_t stream) {
  Args a;
  a.x = (const unsigned short*)d_in[0];
  a.kc = (const unsigned short*)d_in[1];
  a.vc = (const unsigned short*)d_in[2];
  a.wq = (const unsigned short*)d_in[3];
  a.wo = (const unsigned short*)d_in[4];
  a.w1 = (const unsigned short*)d_in[6];
  a.b1 = (const unsigned short*)d_in[7];
  a.w2 = (const unsigned short*)d_in[8];
  a.b2 = (const unsigned short*)d_in[9];
  a.out = (unsigned short*)d_out;
  a.ws = (unsigned char*)d_ws;
  fused_block<<<256, 640, 0, stream>>>(a);
}
```
